```python
import torch

def _softmax(x, axis=-1):
    return torch.softmax(x, dim=axis)
def _gelu(x):
    return torch.nn.functional.gelu(x, approximate="tanh")
def _silu(x):
    return torch.nn.functional.silu(x)
def _sigmoid(x):
    return torch.sigmoid(x)
def _layernorm(x, axis=-1, eps=1e-5):
    m = x.mean(dim=axis, keepdim=True)
    v = x.var(dim=axis, keepdim=True, unbiased=False)
    return (x - m) / torch.sqrt(v + eps)
def _scatter_add(tgt, idx, src, axis=0):
    return tgt.clone().index_add_(axis, idx, src)
def _segment_sum(src, seg, n):
    out = torch.zeros((n,) + src.shape[1:], dtype=src.dtype)
    return out.index_add_(0, seg, src)
def _pad(x, pw):
    flat = tuple(v for pair in reversed(pw) for v in pair)
    return torch.nn.functional.pad(x, flat)

kernel_name = 'recurrent_scan__linear_kernel_hedgehog__dense_activation_mish__prompt'

def setup_inputs():

    _w = (lambda a: (lambda t: t.to(torch.bfloat16) if t.is_floating_point() else t)(torch.as_tensor(a)))
    def _A_setup():
        B, S, D, H = 1, 2048, 576, 32
        Dh = D // H
        return {
            "x": _w((lambda *s: torch.randn(*s, dtype=torch.bfloat16) * 0.05)(B, S, D)),
            "wqkv": _w((lambda *s: torch.randn(*s, dtype=torch.bfloat16) * 0.4)(D, 3 * D)),
            "w_phi": _w((lambda *s: torch.randn(*s, dtype=torch.bfloat16) * 0.4)(H, Dh, Dh) * (Dh ** -0.5)),
            "wo": _w((lambda *s: torch.randn(*s, dtype=torch.bfloat16) * 0.2)(D, D)),
        }
    _A_in = _A_setup()

    def _B_setup():
        B, S, D, F = 1, 2048, 576, 4608
        return {
            "x": _w((lambda *s: torch.randn(*s, dtype=torch.bfloat16) * 0.05)(B, S, D)),
            "w1": _w((lambda *s: torch.randn(*s, dtype=torch.bfloat16) * 0.4)(D, F) * (D ** -0.5)),
            "b1": _w((lambda *s: torch.randn(*s, dtype=torch.bfloat16) * 0.4)(F)),
            "w2": _w((lambda *s: torch.randn(*s, dtype=torch.bfloat16) * 0.1)(F, D) * (F ** -0.5)),
        }
    _B_in = _B_setup()

    return {
        "A_x": _A_in["x"],
        "A_wqkv": _A_in["wqkv"],
        "A_w_phi": _A_in["w_phi"],
        "A_wo": _A_in["wo"],
        "B_x": _B_in["x"],
        "B_w1": _B_in["w1"],
        "B_b1": _B_in["b1"],
        "B_w2": _B_in["w2"],
    }

def reference(A_x, A_wqkv, A_w_phi, A_wo, B_x, B_w1, B_b1, B_w2):
    def _A_ref(x, wqkv, w_phi, wo):
        B, S, D = x.shape
        H = 32; Dh = D // H
        qkv = (lambda x, *d: x.permute(*d))((x @ wqkv).reshape(B, S, 3, H, Dh), 2, 0, 3, 1, 4)
        q, k, v = qkv[0], qkv[1], qkv[2]
        pq = torch.einsum("bhsd,hdf->bhsf", q, w_phi)
        pk = torch.einsum("bhsd,hdf->bhsf", k, w_phi)
        phi_q = torch.concatenate([_softmax(pq, axis=-1), _softmax(-pq, axis=-1)], axis=-1)
        phi_k = torch.concatenate([_softmax(pk, axis=-1), _softmax(-pk, axis=-1)], axis=-1)
        kv = torch.einsum("bhsf,bhsd->bhfd", phi_k, v)
        z  = torch.einsum("bhsf->bhf", phi_k)
        num = torch.einsum("bhqf,bhfd->bhqd", phi_q, kv)
        den = torch.einsum("bhqf,bhf->bhq", phi_q, z)[..., None] + 1e-6
        o = num / den
        return (lambda x, *d: x.permute(*d))(o, 0, 2, 1, 3).reshape(B, S, D) @ wo

    def _B_ref(x, w1, b1, w2):
        h = torch.einsum("bsd,df->bsf", x, w1) + b1
        sp = torch.log1p(torch.exp(-torch.abs(h))) + torch.where(h > 0, h, torch.zeros_like(h))
        act = h * torch.tanh(sp)
        return torch.einsum("bsf,fd->bsd", act, w2)

    x = A_x
    xn = _layernorm(x, axis=-1)
    s = _A_ref(xn, A_wqkv, A_w_phi, A_wo)
    g = _silu(_B_ref(xn, B_w1, B_b1, B_w2))
    return x + s * g
```

```cpp
#include <hip/hip_runtime.h>
#include <hip/hip_bf16.h>
#include <stdint.h>


typedef __attribute__((ext_vector_type(8))) __bf16 bf16x8;
typedef __attribute__((ext_vector_type(2))) __bf16 bf16x2;
typedef __attribute__((ext_vector_type(4))) short s16x4;
typedef __attribute__((ext_vector_type(2))) float f32x2;
typedef __attribute__((ext_vector_type(4))) float f32x4;
typedef __attribute__((ext_vector_type(16))) float f32x16;
typedef __attribute__((ext_vector_type(4))) unsigned u32x4;
typedef __attribute__((ext_vector_type(2))) unsigned u32x2;
typedef unsigned short u16;
#define LDS_AS __attribute__((address_space(3)))
#define DEVI __device__ __forceinline__

constexpr int SEQ = 2048, DM = 576, FF = 4608;
constexpr int XA_OFF = 0;
constexpr int RING_OFF = 73728;
constexpr int SLOT_STRIDE = 36864;
constexpr int S2_OFF = RING_OFF + SLOT_STRIDE;
constexpr int S2_ROW = 464;
constexpr int WPHI_OFF = 147456;
constexpr int B1_OFF = WPHI_OFF + 3072;
constexpr int DUMMY_OFF = B1_OFF + 2048;
constexpr int LDS_BYTES = 163840;
constexpr size_t WS_P = 0;
constexpr size_t WS_KVP = WS_P + (size_t)8 * 64 * 36 * 1024;
constexpr size_t WS_KV = WS_KVP + (size_t)32 * 32 * 800 * 4;
constexpr size_t WS_PQ = WS_KV + (size_t)32 * 800 * 4;
constexpr size_t WS_O = WS_PQ + (size_t)256 * 512 * 40;
constexpr size_t WS_WOT = WS_O + (size_t)64 * 36 * 1024;

DEVI unsigned pk2(float a, float b) {
  f32x2 v = {a, b};
  return __builtin_bit_cast(unsigned, __builtin_convertvector(v, bf16x2));
}
DEVI float bflo(unsigned u) { return __builtin_bit_cast(float, u << 16); }
DEVI float bfhi(unsigned u) { return __builtin_bit_cast(float, u & 0xffff0000u); }
DEVI u32x2 trd(unsigned a) {
  return __builtin_bit_cast(u32x2, __builtin_amdgcn_ds_read_tr16_b64_v4i16((LDS_AS s16x4*)(uintptr_t)a));
}
DEVI u32x4 lrd128(unsigned a) { return *(LDS_AS u32x4*)(uintptr_t)a; }
DEVI u32x2 lrd64(unsigned a) { return *(LDS_AS u32x2*)(uintptr_t)a; }
DEVI void lwr128(unsigned a, u32x4 v) { *(LDS_AS u32x4*)(uintptr_t)a = v; }
DEVI void lwr64(unsigned a, u32x2 v) { *(LDS_AS u32x2*)(uintptr_t)a = v; }
DEVI f32x4 mma16(u32x4 a, u32x4 b, f32x4 c) {
  return __builtin_amdgcn_mfma_f32_16x16x32_bf16(__builtin_bit_cast(bf16x8, a), __builtin_bit_cast(bf16x8, b), c, 0, 0, 0);
}
DEVI f32x16 mma32(u32x4 a, u32x4 b, f32x16 c) {
  return __builtin_amdgcn_mfma_f32_32x32x16_bf16(__builtin_bit_cast(bf16x8, a), __builtin_bit_cast(bf16x8, b), c, 0, 0, 0);
}
DEVI void dma16(unsigned ldsb, unsigned voff, const char* base) {
  asm volatile("s_mov_b32 m0, %0\n\ts_nop 0\n\tglobal_load_lds_dwordx4 %1, %2" ::"s"(ldsb), "v"(voff), "s"(base) : "memory");
}
#define WAIT_V(n) asm volatile("s_waitcnt vmcnt(%0)" ::"i"(n) : "memory")
#define WAIT_L0() asm volatile("s_waitcnt lgkmcnt(0)" ::: "memory")
#define BAR() asm volatile("s_waitcnt lgkmcnt(0)\n\ts_barrier" ::: "memory")
DEVI float xhalf(float v) {
  return __builtin_bit_cast(float, __builtin_amdgcn_ds_bpermute((int)(((threadIdx.x & 63) ^ 32) << 2), __builtin_bit_cast(int, v)));
}

template <int NT>
DEVI void issue_slab(unsigned slot, const char* rowbase, int ldb, unsigned vmain, unsigned vext) {
#pragma unroll
  for (int i = 0; i < 4; ++i) dma16(slot + i * 1024, vmain, rowbase + (size_t)i * 8 * ldb);
  if (NT == 5) dma16(slot + 4096, vext, rowbase);
}

template <int NT>
DEVI void gemm_step(f32x4 (&acc)[NT][4], const unsigned (&ta)[5], unsigned soff, unsigned xaddr, unsigned slot,
                    bool do_issue, const char* nrow, int nld, unsigned vmain, unsigned vext) {
  u32x4 wf[NT], xf[4];
#pragma unroll
  for (int t = 0; t < NT; ++t) {
    u32x2 lo = trd(ta[t] + soff), hi = trd(ta[t] + soff + (t < 4 ? 2048 : 512));
    wf[t] = u32x4{lo.x, lo.y, hi.x, hi.y};
  }
#pragma unroll
  for (int m = 0; m < 4; ++m) xf[m] = lrd128(xaddr + m * 1024);
  WAIT_L0();
  if (do_issue) issue_slab<NT>(slot, nrow, nld, vmain, vext);
#pragma unroll
  for (int t = 0; t < NT; ++t)
#pragma unroll
    for (int m = 0; m < 4; ++m) acc[t][m] = mma16(wf[t], xf[m], acc[t][m]);
}

DEVI float mish(float h) {
  float e = __builtin_amdgcn_exp2f(h * 1.4426950408889634f);
  float d = __builtin_fmaf(e, e + 2.0f, 2.0f);
  float r = __builtin_amdgcn_rcpf(d);
  return h * __builtin_fmaf(-2.0f, r, 1.0f);
}

template <int NT>
DEVI void mlp(int w, int lane, int i, int j, const char* w1, const char* w2, char* P) {
  const int c16 = lane & 15, g = lane >> 4;
  const unsigned slot0 = RING_OFF + (w < 4 ? w * 5120 : 20480 + (w - 4) * 4096);
  const unsigned chs = ((lane & 7) ^ (((lane >> 4) & 3) << 1)) * 16;
  const unsigned v1m = (lane >> 3) * (FF * 2) + w * 128 + chs, v1e = (lane >> 1) * (FF * 2) + 1024 + w * 32 + (lane & 1) * 16;
  const unsigned v2m = (lane >> 3) * (DM * 2) + w * 128 + chs, v2e = (lane >> 1) * (DM * 2) + 1024 + w * 32 + (lane & 1) * 16;
  const char* b1p = w1 + (size_t)j * 1152;
  const char* b2p = w2 + (size_t)j * 576 * 1152;
  unsigned ta[5];
  {
    const int q = (lane >> 2) & 3, p = lane & 3, r = 4 * g + q, s = (r >> 1) & 3;
#pragma unroll
    for (int t = 0; t < 4; ++t) ta[t] = slot0 + 128 * r + 32 * (t ^ s) + 8 * p;
    ta[4] = slot0 + 4096 + 32 * r + 8 * p;
  }
  const unsigned xr0 = XA_OFF + (g * 16 + (c16 ^ (g >> 1))) * 16, xr1 = XA_OFF + 4096 + (g * 16 + (c16 ^ (2 + (g >> 1)))) * 16;
  issue_slab<NT>(slot0 + SLOT_STRIDE, b1p + (size_t)32 * FF * 2, FF * 2, v1m, v1e);
  f32x4 acc[NT][4];
#pragma unroll
  for (int t = 0; t < NT; ++t) {
    u32x2 b = lrd64(B1_OFF + (t < 4 ? w * 128 + t * 32 : 1024 + w * 32) + g * 8);
    f32x4 bv = {bflo(b.x), bfhi(b.x), bflo(b.y), bfhi(b.y)};
#pragma unroll
    for (int m = 0; m < 4; ++m) acc[t][m] = bv;
  }
  for (int ks = 0; ks < 16; ks += 2) {
    WAIT_V(NT);
    gemm_step<NT>(acc, ta, 0, xr0 + ks * 4096, slot0, true, b1p + (size_t)(ks + 2) * 32 * FF * 2, FF * 2, v1m, v1e);
    WAIT_V(NT);
    gemm_step<NT>(acc, ta, SLOT_STRIDE, xr1 + ks * 4096, slot0 + SLOT_STRIDE, true, b1p + (size_t)(ks + 3) * 32 * FF * 2, FF * 2, v1m, v1e);
  }
  WAIT_V(NT);
  gemm_step<NT>(acc, ta, 0, xr0 + 16 * 4096, slot0, true, b2p, DM * 2, v2m, v2e);
  WAIT_V(NT);
  gemm_step<NT>(acc, ta, SLOT_STRIDE, xr1 + 16 * 4096, slot0 + SLOT_STRIDE, true, b2p + 32 * DM * 2, DM * 2, v2m, v2e);
  u32x2 act[NT][4];
#pragma unroll
  for (int t = 0; t < NT; ++t)
#pragma unroll
    for (int m = 0; m < 4; ++m) {
      act[t][m].x = pk2(mish(acc[t][m][0]), mish(acc[t][m][1]));
      act[t][m].y = pk2(mish(acc[t][m][2]), mish(acc[t][m][3]));
      acc[t][m] = f32x4{0.f, 0.f, 0.f, 0.f};
    }
  BAR();
#pragma unroll
  for (int m = 0; m < 4; ++m) {
    lwr128(xr0 + w * 8192 + m * 1024, u32x4{act[0][m].x, act[0][m].y, act[1][m].x, act[1][m].y});
    lwr128(xr1 + w * 8192 + m * 1024, u32x4{act[2][m].x, act[2][m].y, act[3][m].x, act[3][m].y});
    if (NT == 5) lwr64(((w & 2) ? xr1 : xr0) + 16 * 4096 + m * 1024 + (w & 1) * 8, act[NT - 1][m]);
  }
  BAR();
  for (int ks = 0; ks < 16; ks += 2) {
    WAIT_V(NT);
    gemm_step<NT>(acc, ta, 0, xr0 + ks * 4096, slot0, true, b2p + (size_t)(ks + 2) * 32 * DM * 2, DM * 2, v2m, v2e);
    WAIT_V(NT);
    gemm_step<NT>(acc, ta, SLOT_STRIDE, xr1 + ks * 4096, slot0 + SLOT_STRIDE, true, b2p + (size_t)(ks + 3) * 32 * DM * 2, DM * 2, v2m, v2e);
  }
  WAIT_V(NT);
  gemm_step<NT>(acc, ta, 0, xr0 + 16 * 4096, slot0, false, b2p, 0, v2m, v2e);
  WAIT_V(0);
  gemm_step<NT>(acc, ta, SLOT_STRIDE, xr1 + 16 * 4096, slot0, false, b2p, 0, v2m, v2e);
#pragma unroll
  for (int t = 0; t < NT; ++t) {
    const int nt = t < 4 ? w * 4 + t : 32 + w;
#pragma unroll
    for (int h = 0; h < 2; ++h) {
      u32x4 o = {pk2(acc[t][2 * h][0], acc[t][2 * h][1]), pk2(acc[t][2 * h][2], acc[t][2 * h][3]),
                 pk2(acc[t][2 * h + 1][0], acc[t][2 * h + 1][1]), pk2(acc[t][2 * h + 1][2], acc[t][2 * h + 1][3])};
      *(u32x4*)(P + ((size_t)((j * 64 + 2 * i + h) * 36 + nt) * 64 + lane) * 16) = o;
    }
  }
}

DEVI void hedgehog(const f32x16& p, unsigned (&out)[10]) {
  float mx = p[0], mn = p[0];
#pragma unroll
  for (int r = 1; r < 9; ++r) { mx = fmaxf(mx, p[r]); mn = fminf(mn, p[r]); }
  mx = fmaxf(mx, xhalf(mx));
  mn = fminf(mn, xhalf(mn));
  const float L2E = 1.4426950408889634f;
  float e[20], sp = 0.f, sn = 0.f;
#pragma unroll
  for (int r = 0; r < 9; ++r) {
    e[r] = __builtin_amdgcn_exp2f((p[r] - mx) * L2E);
    e[9 + r] = __builtin_amdgcn_exp2f((mn - p[r]) * L2E);
    sp += e[r]; sn += e[9 + r];
  }
  sp += xhalf(sp); sn += xhalf(sn);
  const float ip = __builtin_amdgcn_rcpf(sp), in = __builtin_amdgcn_rcpf(sn);
#pragma unroll
  for (int r = 0; r < 9; ++r) { e[r] *= ip; e[9 + r] *= in; }
#pragma unroll
  for (int r = 0; r < 9; ++r) out[r] = pk2(e[2 * r], e[2 * r + 1]);
  out[9] = 0u;
}

__global__ __launch_bounds__(512, 2) void k1(const u16* __restrict__ x, const u16* __restrict__ wqkv, const u16* __restrict__ wphi,
                                             const u16* __restrict__ wo, const u16* __restrict__ w1, const u16* __restrict__ b1,
                                             const u16* __restrict__ w2, char* __restrict__ ws) {
  __shared__ __attribute__((aligned(16))) char smem[LDS_BYTES];
  asm volatile("" ::"v"((unsigned)(uintptr_t)(LDS_AS char*)smem));
  const int tid = threadIdx.x, lane = tid & 63, w = __builtin_amdgcn_readfirstlane(tid >> 6);
  const int j = blockIdx.x & 7, i = blockIdx.x >> 3;
  const int c16 = lane & 15, g = lane >> 4;
  const unsigned slot0 = RING_OFF + (w < 4 ? w * 5120 : 20480 + (w - 4) * 4096);
  const int r8 = lane >> 3, sub = lane & 7;
  const int mrow = w * 8 + ((r8 >> 1) | ((r8 & 1) << 2));
  u32x4 xv[9];
  {
    const unsigned vo = mrow * 1152 + sub * 16;
    const char* xb = (const char*)x + (size_t)i * 64 * 1152;
#pragma unroll
    for (int c = 0; c < 9; ++c) asm volatile("global_load_dwordx4 %0, %1, %2 offset:%3" : "=v"(xv[c]) : "v"(vo), "s"(xb), "i"(c * 128) : "memory");
  }
  unsigned vq;
  {
    int c = 4 * w + ((lane & 3) ^ (((lane >> 4) & 1) << 1));
    c = c > 26 ? 26 : c;
    const int part = c / 9, cc = c - part * 9;
    vq = (lane >> 2) * 3456 + (part == 0 ? 2304 : (part - 1) * 1152) + j * 144 + cc * 16;
  }
  const char* wq = (const char*)wqkv;
  if (w < 7) {
#pragma unroll
    for (int s = 0; s < 4; ++s) {
      const unsigned sl = slot0 + (s & 1) * SLOT_STRIDE + (s >> 1) * 2048;
      dma16(sl, vq, wq + (size_t)s * 32 * 3456);
      dma16(sl + 1024, vq, wq + (size_t)(s * 32 + 16) * 3456);
    }
  } else {
#pragma unroll
    for (int s = 0; s < 3; ++s) { int c = s * 64 + lane; c = c > 161 ? 161 : c; dma16(WPHI_OFF + s * 1024, c * 16, (const char*)wphi + j * 2592); }
#pragma unroll
    for (int s = 0; s < 2; ++s) { int c = s * 64 + lane; c = c > 71 ? 71 : c; dma16(B1_OFF + s * 1024, c * 16, (const char*)b1 + j * 1152); }
    dma16(DUMMY_OFF, lane * 16, (const char*)x); dma16(DUMMY_OFF, lane * 16, (const char*)x); dma16(DUMMY_OFF, lane * 16, (const char*)x);
  }
  {
    int c = tid > 485 ? 485 : tid;
    int r = c / 27, cc = c - r * 27, part = cc / 9;
    dma16(DUMMY_OFF, (i * 18 + r) * 3456 + part * 1152 + j * 144 + (cc - part * 9) * 16, wq);
#pragma unroll
    for (int s = 0; s < 3; ++s) {
      c = s * 512 + tid; c = c > 1295 ? 1295 : c;
      r = c / 72; cc = c - r * 72;
      dma16(DUMMY_OFF, (i * 18 + r) * 9216 + j * 1152 + cc * 16, (const char*)w1);
    }
#pragma unroll
    for (int s = 0; s < 3; ++s) {
      c = s * 512 + tid; c = c > 1295 ? 1295 : c;
      dma16(DUMMY_OFF, (j * 576 + i * 18) * 1152 + c * 16, (const char*)w2);
    }
  }
  asm volatile("s_waitcnt vmcnt(15)" : "+v"(xv[0]), "+v"(xv[1]), "+v"(xv[2]), "+v"(xv[3]), "+v"(xv[4]), "+v"(xv[5]), "+v"(xv[6]), "+v"(xv[7]), "+v"(xv[8])::"memory");
  {
    float s = 0.f;
#pragma unroll
    for (int c = 0; c < 9; ++c)
#pragma unroll
      for (int e = 0; e < 4; ++e) s += bflo(xv[c][e]) + bfhi(xv[c][e]);
    s += __shfl_xor(s, 1); s += __shfl_xor(s, 2); s += __shfl_xor(s, 4);
    const float mean = s * (1.0f / 576.0f);
    float v = 0.f;
#pragma unroll
    for (int c = 0; c < 9; ++c)
#pragma unroll
      for (int e = 0; e < 4; ++e) { float a = bflo(xv[c][e]) - mean, b = bfhi(xv[c][e]) - mean; v += a * a; v += b * b; }
    v += __shfl_xor(v, 1); v += __shfl_xor(v, 2); v += __shfl_xor(v, 4);
    const float rs = __builtin_amdgcn_rsqf(v * (1.0f / 576.0f) + 1e-5f), nb = -mean * rs;
    const int f = ((sub >> 2) << 1) + (sub & 1);
    const unsigned wa = XA_OFF + (sub >> 2) * 4096 + (mrow >> 4) * 1024 + ((sub & 1) * 32 + ((mrow & 15) ^ f)) * 16 + ((sub >> 1) & 1) * 8;
#pragma unroll
    for (int c = 0; c < 9; ++c) {
      unsigned o[4];
#pragma unroll
      for (int e = 0; e < 4; ++e) o[e] = pk2(__builtin_fmaf(bflo(xv[c][e]), rs, nb), __builtin_fmaf(bfhi(xv[c][e]), rs, nb));
      lwr64(wa + c * 8192, u32x2{o[0], o[1]});
      lwr64(wa + c * 8192 + 256, u32x2{o[2], o[3]});
    }
  }
  if (w == 7) WAIT_V(7);
  BAR();
  const unsigned xr0 = XA_OFF + (g * 16 + (c16 ^ (g >> 1))) * 16, xr1 = XA_OFF + 4096 + (g * 16 + (c16 ^ (2 + (g >> 1)))) * 16;
  f32x4 qa[2][4];
#pragma unroll
  for (int t = 0; t < 2; ++t)
#pragma unroll
    for (int m = 0; m < 4; ++m) qa[t][m] = f32x4{0.f, 0.f, 0.f, 0.f};
  if (w < 7) {
    const int q = (lane >> 2) & 3, p = lane & 3, r = 4 * g + q;
    const unsigned t0 = slot0 + 64 * r + 32 * (g & 1) + 8 * p, t1 = slot0 + 64 * r + 32 * (1 ^ (g & 1)) + 8 * p;
#pragma unroll
    for (int ks = 0; ks < 18; ++ks) {
      if (ks < 4) WAIT_V(13); else if (ks < 15) WAIT_V(6); else if (ks == 15) WAIT_V(4); else if (ks == 16) WAIT_V(2); else WAIT_V(0);
      const unsigned so = (ks & 1) * SLOT_STRIDE + ((ks >> 1) & 1) * 2048;
      u32x2 a0 = trd(t0 + so), a1 = trd(t0 + so + 1024), b0 = trd(t1 + so), b1_ = trd(t1 + so + 1024);
      u32x4 xf[4];
#pragma unroll
      for (int m = 0; m < 4; ++m) xf[m] = lrd128(((ks & 1) ? xr1 : xr0) + (ks >> 1) * 8192 + m * 1024);
      WAIT_L0();
      if (ks + 4 < 18) {
        dma16(slot0 + so, vq, wq + (size_t)(ks + 4) * 32 * 3456);
        dma16(slot0 + so + 1024, vq, wq + (size_t)((ks + 4) * 32 + 16) * 3456);
      }
      const u32x4 wa = {a0.x, a0.y, a1.x, a1.y}, wb = {b0.x, b0.y, b1_.x, b1_.y};
#pragma unroll
      for (int m = 0; m < 4; ++m) { qa[0][m] = mma16(wa, xf[m], qa[0][m]); qa[1][m] = mma16(wb, xf[m], qa[1][m]); }
    }
  } else {
#pragma unroll
    for (int s = 0; s < 3; ++s) {
      const int fr = blockIdx.x + s * 256;
      if (fr < 648) {
        const int ks = fr / 36, nt = fr - ks * 36;
        const u16* src = wo + (ks * 32 + g * 8) * 576 + nt * 16 + c16;
        u32x4 o;
#pragma unroll
        for (int e = 0; e < 4; ++e) o[e] = (unsigned)src[(2 * e) * 576] | ((unsigned)src[(2 * e + 1) * 576] << 16);
        *(u32x4*)(ws + WS_WOT + ((size_t)fr * 64 + lane) * 16) = o;
      }
    }
    WAIT_V(0);
  }
  {
    const unsigned chs = ((lane & 7) ^ (((lane >> 4) & 3) << 1)) * 16;
    const unsigned v1m = (lane >> 3) * (FF * 2) + w * 128 + chs, v1e = (lane >> 1) * (FF * 2) + 1024 + w * 32 + (lane & 1) * 16;
    if (w < 4) issue_slab<5>(slot0, (const char*)w1 + j * 1152, FF * 2, v1m, v1e);
    else issue_slab<4>(slot0, (const char*)w1 + j * 1152, FF * 2, v1m, v1e);
  }
  BAR();
  if (w < 7) {
#pragma unroll
    for (int t = 0; t < 2; ++t)
#pragma unroll
      for (int m = 0; m < 4; ++m)
        lwr64(S2_OFF + (m * 16 + c16) * S2_ROW + (2 * w + t) * 32 + g * 8, u32x2{pk2(qa[t][m][0], qa[t][m][1]), pk2(qa[t][m][2], qa[t][m][3])});
  }
  const int hh = w & 3, mh = w >> 2, r32 = lane & 31, hf = lane >> 5;
  u32x4 wp[2];
  {
    const int reg = (r32 & 3) + 4 * (r32 >> 3), f = 9 * ((r32 >> 2) & 1) + reg;
#pragma unroll
    for (int s = 0; s < 2; ++s)
#pragma unroll
      for (int e = 0; e < 4; ++e) {
        unsigned v2[2];
#pragma unroll
        for (int b = 0; b < 2; ++b) {
          const int d = 16 * s + 8 * hf + 2 * e + b - 2 * hh;
          const bool ok = reg < 9 && d >= 0 && d < 18;
          const unsigned val = *(LDS_AS u16*)(uintptr_t)(WPHI_OFF + (ok ? (hh * 324 + d * 18 + f) * 2 : 0));
          v2[b] = ok ? val : 0u;
        }
        wp[s][e] = v2[0] | (v2[1] << 16);
      }
  }
  BAR();
  const unsigned rowa = S2_OFF + (mh * 32 + r32) * S2_ROW;
  u32x4 qf[2], kf[2];
#pragma unroll
  for (int s = 0; s < 2; ++s) { qf[s] = lrd128(rowa + 144 + hh * 32 + s * 32 + hf * 16); kf[s] = lrd128(rowa + 288 + hh * 32 + s * 32 + hf * 16); }
  BAR();
  unsigned pq[10];
  {
    f32x16 z = {0.f, 0.f, 0.f, 0.f, 0.f, 0.f, 0.f, 0.f, 0.f, 0.f, 0.f, 0.f, 0.f, 0.f, 0.f, 0.f};
    f32x16 pk = mma32(wp[1], kf[1], mma32(wp[0], kf[0], z));
    f32x16 pqa = mma32(wp[1], qf[1], mma32(wp[0], qf[0], z));
    unsigned ph[10];
    hedgehog(pk, ph);
#pragma unroll
    for (int e = 0; e < 5; ++e) lwr64(rowa + 144 + hh * 80 + hf * 40 + e * 8, u32x2{ph[2 * e], ph[2 * e + 1]});
    hedgehog(pqa, pq);
  }
  BAR();
  {
    const int q = (lane >> 2) & 3, p = lane & 3;
    const unsigned ra = S2_OFF + (8 * g + q) * S2_ROW + 8 * p;
    f32x4 kv[3] = {{0.f, 0.f, 0.f, 0.f}, {0.f, 0.f, 0.f, 0.f}, {0.f, 0.f, 0.f, 0.f}};
    const int col = mh * 16 + c16;
#pragma unroll
    for (int s = 0; s < 2; ++s) {
      const unsigned a = ra + s * 32 * S2_ROW;
      u32x2 v0 = trd(a + hh * 32 + mh * 32), v1 = trd(a + 4 * S2_ROW + hh * 32 + mh * 32);
      u32x4 vf = {v0.x, v0.y, v1.x, v1.y};
      if (col == 31) vf = u32x4{0x3f803f80u, 0x3f803f80u, 0x3f803f80u, 0x3f803f80u};
#pragma unroll
      for (int ft = 0; ft < 3; ++ft) {
        u32x2 p0 = trd(a + 144 + hh * 80 + ft * 32), p1 = trd(a + 4 * S2_ROW + 144 + hh * 80 + ft * 32);
        kv[ft] = mma16(u32x4{p0.x, p0.y, p1.x, p1.y}, vf, kv[ft]);
      }
    }
    const int d = col == 31 ? 18 : col - 2 * hh;
    if (d >= 0 && (d < 18 || col == 31)) {
      float* dst = (float*)(ws + WS_KVP) + ((size_t)((j * 4 + hh) * 32 + i) * 20 + d) * 40 + 4 * g;
      *(f32x4*)dst = kv[0];
      *(f32x4*)(dst + 16) = kv[1];
      if (g < 2) *(f32x4*)(dst + 32) = kv[2];
    }
    unsigned* pd = (unsigned*)(ws + WS_PQ) + ((size_t)blockIdx.x * 512 + tid) * 10;
#pragma unroll
    for (int e = 0; e < 5; ++e) *(u32x2*)(pd + 2 * e) = u32x2{pq[2 * e], pq[2 * e + 1]};
  }
  BAR();
  WAIT_V(0);
  if (w < 4) mlp<5>(w, lane, i, j, (const char*)w1, (const char*)w2, ws + WS_P);
  else mlp<4>(w, lane, i, j, (const char*)w1, (const char*)w2, ws + WS_P);
}

__global__ __launch_bounds__(256) void kred(char* __restrict__ ws) {
  const int e = blockIdx.x * 256 + threadIdx.x;
  const int h = e / 800, r = e - h * 800;
  const float* src = (const float*)(ws + WS_KVP) + (size_t)h * 32 * 800 + r;
  float s = 0.f;
  if (r < 760)
    for (int t = 0; t < 32; ++t) s += src[t * 800];
  ((float*)(ws + WS_KV))[e] = s;
}

__global__ __launch_bounds__(512) void ko(char* __restrict__ ws) {
  __shared__ __attribute__((aligned(16))) u16 ol[64 * 72];
  const int tid = threadIdx.x, lane = tid & 63, w = tid >> 6;
  const int j = blockIdx.x & 7, i = blockIdx.x >> 3;
  const int hh = w & 3, mh = w >> 2, r32 = lane & 31, hf = lane >> 5;
  unsigned pq[12];
  const unsigned* pd = (const unsigned*)(ws + WS_PQ) + ((size_t)blockIdx.x * 512 + tid) * 10;
  for (int e = 0; e < 10; ++e) pq[e] = pd[e];
  pq[10] = pq[11] = 0u;
  const int reg = (r32 & 3) + 4 * (r32 >> 3);
  const int d = reg < 9 ? 9 * ((r32 >> 2) & 1) + reg : (reg == 9 ? 18 : 19);
  const float* kv = (const float*)(ws + WS_KV) + ((j * 4 + hh) * 20 + d) * 40 + hf * 20;
  f32x16 acc = {0.f, 0.f, 0.f, 0.f, 0.f, 0.f, 0.f, 0.f, 0.f, 0.f, 0.f, 0.f, 0.f, 0.f, 0.f, 0.f};
#pragma unroll
  for (int s = 0; s < 3; ++s) {
    u32x4 a;
#pragma unroll
    for (int e = 0; e < 4; ++e) a[e] = (8 * s + 2 * e < 20) ? pk2(kv[8 * s + 2 * e], kv[8 * s + 2 * e + 1]) : 0u;
    acc = mma32(a, u32x4{pq[4 * s], pq[4 * s + 1], pq[4 * s + 2], pq[4 * s + 3]}, acc);
  }
  const float inv = 1.0f / (acc[9] + 1e-6f);
#pragma unroll
  for (int r = 0; r < 9; ++r) ol[(mh * 32 + r32) * 72 + hh * 18 + hf * 9 + r] = (u16)(pk2(acc[r] * inv, 0.f) & 0xffffu);
  __syncthreads();
  for (int c = tid; c < 576; c += 512) {
    const int m = c & 63, cc = c >> 6, ch = j * 9 + cc;
    const u32x4 v = *(const u32x4*)(ol + m * 72 + cc * 8);
    *(u32x4*)(ws + WS_O + ((size_t)(((2 * i + (m >> 5)) * 18 + (ch >> 2)) * 2 + ((m >> 4) & 1)) * 64 + (ch & 3) * 16 + (m & 15)) * 16) = v;
  }
}

__global__ __launch_bounds__(576) void k2(const u16* __restrict__ x, const char* __restrict__ ws, u16* __restrict__ out) {
  __shared__ __attribute__((aligned(16))) char ob[36864];
  const int tid = threadIdx.x, lane = tid & 63, w = __builtin_amdgcn_readfirstlane(tid >> 6);
  const int b = blockIdx.x, i32 = (b & 7) + 8 * (b >> 5), nt = ((b >> 3) & 3) * 9 + w;
  const int c16 = lane & 15, g = lane >> 4;
  const unsigned obase = (unsigned)(uintptr_t)(LDS_AS char*)ob;
#pragma unroll
  for (int s = 0; s < 4; ++s) dma16(obase + (w * 4 + s) * 1024, lane * 16, ws + WS_O + ((size_t)i32 * 36 + w * 4 + s) * 1024);
  u32x4 wf[18], pp[8];
  u32x2 xr[2];
#pragma unroll
  for (int ks = 0; ks < 18; ++ks) wf[ks] = *(const u32x4*)(ws + WS_WOT + ((size_t)(ks * 36 + nt) * 64 + lane) * 16);
#pragma unroll
  for (int s = 0; s < 8; ++s) pp[s] = *(const u32x4*)(ws + WS_P + ((size_t)((s * 64 + i32) * 36 + nt) * 64 + lane) * 16);
#pragma unroll
  for (int m = 0; m < 2; ++m) xr[m] = *(const u32x2*)(x + (size_t)(i32 * 32 + m * 16 + c16) * 576 + nt * 16 + g * 4);
  WAIT_V(28);
  BAR();
  f32x4 acc[2] = {{0.f, 0.f, 0.f, 0.f}, {0.f, 0.f, 0.f, 0.f}};
#pragma unroll
  for (int ks = 0; ks < 18; ++ks)
#pragma unroll
    for (int m = 0; m < 2; ++m) acc[m] = mma16(wf[ks], lrd128(obase + (ks * 2 + m) * 1024 + lane * 16), acc[m]);
#pragma unroll
  for (int m = 0; m < 2; ++m) {
    float bs[4] = {0.f, 0.f, 0.f, 0.f};
#pragma unroll
    for (int s = 0; s < 8; ++s) {
      bs[0] += bflo(pp[s][2 * m]); bs[1] += bfhi(pp[s][2 * m]); bs[2] += bflo(pp[s][2 * m + 1]); bs[3] += bfhi(pp[s][2 * m + 1]);
    }
    const float xs[4] = {bflo(xr[m].x), bfhi(xr[m].x), bflo(xr[m].y), bfhi(xr[m].y)};
    float o[4];
#pragma unroll
    for (int r = 0; r < 4; ++r) {
      const float sg = __builtin_amdgcn_rcpf(1.0f + __builtin_amdgcn_exp2f(-1.4426950408889634f * bs[r]));
      o[r] = __builtin_fmaf(acc[m][r], bs[r] * sg, xs[r]);
    }
    *(u32x2*)(out + (size_t)(i32 * 32 + m * 16 + c16) * 576 + nt * 16 + g * 4) = u32x2{pk2(o[0], o[1]), pk2(o[2], o[3])};
  }
}

extern "C" void kernel_launch(void* const* d_in, const int* in_sizes, int n_in, void* d_out, int out_size, void* d_ws, size_t ws_size,
                              hipStream_t stream) {
  const u16* x = (const u16*)d_in[0];
  const u16* wqkv = (const u16*)d_in[1];
  const u16* wphi = (const u16*)d_in[2];
  const u16* wo = (const u16*)d_in[3];
  const u16* w1 = (const u16*)d_in[5];
  const u16* b1 = (const u16*)d_in[6];
  const u16* w2 = (const u16*)d_in[7];
  char* ws = (char*)d_ws;
  k1<<<256, 512, 0, stream>>>(x, wqkv, wphi, wo, w1, b1, w2, ws);
  kred<<<100, 256, 0, stream>>>(ws);
  ko<<<256, 512, 0, stream>>>(ws);
  k2<<<256, 576, 0, stream>>>(x, ws, (u16*)d_out);
}
```

```cpp
#include <hip/hip_runtime.h>
#include <hip/hip_bf16.h>
#include <stdint.h>

typedef __attribute__((ext_vector_type(8))) __bf16 bf16x8;
typedef __attribute__((ext_vector_type(2))) __bf16 bf16x2;
typedef __attribute__((ext_vector_type(4))) short s16x4;
typedef __attribute__((ext_vector_type(2))) float f32x2;
typedef __attribute__((ext_vector_type(4))) float f32x4;
typedef __attribute__((ext_vector_type(16))) float f32x16;
typedef __attribute__((ext_vector_type(4))) unsigned u32x4;
typedef __attribute__((ext_vector_type(2))) unsigned u32x2;
typedef unsigned short u16;
#define LDS_AS __attribute__((address_space(3)))
#define DEVI __device__ __forceinline__

constexpr int SEQ = 2048, DM = 576, FF = 4608;
constexpr int XA_OFF = 0;
constexpr int RING_OFF = 73728;
constexpr int S2_OFF = RING_OFF + 32768;
constexpr int S2_ROW = 464;
constexpr int WPHI_OFF = RING_OFF + 65536;
constexpr int PH_OFF = WPHI_OFF + 3072;
constexpr int DUMMY_OFF = PH_OFF + 20480;
constexpr int LDS_BYTES = 163840;
constexpr size_t WS_P = 0;
constexpr size_t WS_KVP = WS_P + (size_t)8 * 64 * 36 * 1024;
constexpr size_t WS_KV = WS_KVP + (size_t)8 * 32 * 1536 * 8;
constexpr size_t WS_EP = WS_KV + (size_t)8 * 1536 * 8;
constexpr size_t WS_O = WS_EP + 1024;
constexpr size_t WS_WOT = WS_O + (size_t)64 * 36 * 1024;

#define STAMP(n)
#define STAMP_DECL
#define STAMP_ARG
#define STAMP_PASS
#define STAMP_FLUSH(ws)
DEVI unsigned pk2(float a, float b) {
  f32x2 v = {a, b};
  return __builtin_bit_cast(unsigned, __builtin_convertvector(v, bf16x2));
}
DEVI float bflo(unsigned u) { return __builtin_bit_cast(float, u << 16); }
DEVI float bfhi(unsigned u) { return __builtin_bit_cast(float, u & 0xffff0000u); }
DEVI u32x2 trd(unsigned a) {
  return __builtin_bit_cast(u32x2, __builtin_amdgcn_ds_read_tr16_b64_v4i16((LDS_AS s16x4*)(uintptr_t)a));
}
DEVI u32x4 lrd128(unsigned a) { return *(LDS_AS u32x4*)(uintptr_t)a; }
DEVI u32x2 lrd64(unsigned a) { return *(LDS_AS u32x2*)(uintptr_t)a; }
DEVI void lwr128(unsigned a, u32x4 v) { *(LDS_AS u32x4*)(uintptr_t)a = v; }
DEVI void lwr64(unsigned a, u32x2 v) { *(LDS_AS u32x2*)(uintptr_t)a = v; }
DEVI f32x4 mma16(u32x4 a, u32x4 b, f32x4 c) {
  return __builtin_amdgcn_mfma_f32_16x16x32_bf16(__builtin_bit_cast(bf16x8, a), __builtin_bit_cast(bf16x8, b), c, 0, 0, 0);
}
DEVI f32x16 mma32(u32x4 a, u32x4 b, f32x16 c) {
  return __builtin_amdgcn_mfma_f32_32x32x16_bf16(__builtin_bit_cast(bf16x8, a), __builtin_bit_cast(bf16x8, b), c, 0, 0, 0);
}
DEVI void dma16(unsigned ldsb, unsigned voff, const char* base) {
  asm volatile("s_mov_b32 m0, %0\n\ts_nop 0\n\tglobal_load_lds_dwordx4 %1, %2" ::"s"(ldsb), "v"(voff), "s"(base) : "memory");
}
#define WAIT_V(n) asm volatile("s_waitcnt vmcnt(%0)" ::"i"(n) : "memory")
#define WAIT_L0() asm volatile("s_waitcnt lgkmcnt(0)" ::: "memory")
#define BAR() asm volatile("s_waitcnt lgkmcnt(0)\n\ts_barrier" ::: "memory")
typedef __attribute__((address_space(1))) unsigned long long gu64;
typedef __attribute__((address_space(1))) unsigned gu32;
DEVI void st_gran(char* p, unsigned tag, unsigned v) {
  __hip_atomic_store((gu64*)p, ((unsigned long long)tag << 32) | v, __ATOMIC_RELAXED, __HIP_MEMORY_SCOPE_AGENT);
}
template <int N>
DEVI void sweep(const char* p, int S, unsigned tag, unsigned (&v)[N]) {
  for (int spins = 0; spins < (1 << 16); ++spins) {
    bool ok = true;
#pragma unroll
    for (int k = 0; k < N; ++k) {
      const unsigned long long x = __hip_atomic_load((gu64*)(p + k * S), __ATOMIC_RELAXED, __HIP_MEMORY_SCOPE_AGENT);
      v[k] = (unsigned)x;
      ok &= (unsigned)(x >> 32) == tag;
    }
    if (__all(ok)) break;
    __builtin_amdgcn_s_sleep(8);
  }
}
#define SCHED() __builtin_amdgcn_sched_barrier(0)
DEVI f32x2 halves(float v) {
  const unsigned u = __builtin_bit_cast(unsigned, v);
  auto r = __builtin_amdgcn_permlane32_swap(u, u, false, false);
  const unsigned r0 = r[0], r1 = r[1];
  return f32x2{__builtin_bit_cast(float, r0), __builtin_bit_cast(float, r1)};
}

struct Strm { const char* base; int ld; unsigned voff; };
DEVI Strm mk_strm(const char* base, int ld, int lane) {
  return Strm{base, ld, (unsigned)((lane >> 3) * ld + ((lane & 7) ^ (((lane >> 4) & 3) << 1)) * 16)};
}
DEVI void issue_slab(unsigned slot, const Strm& s, int ks) {
  const char* rb = s.base + (size_t)(ks * 32 * s.ld);
#pragma unroll
  for (int i = 0; i < 4; ++i) dma16(slot + i * 1024, s.voff, rb + (size_t)(i * 8 * s.ld));
}
struct Frag { u32x4 w[4], x[4]; };
template <bool MK>
DEVI void read_frag(Frag& f, const unsigned (&ta)[4], unsigned soff, unsigned xaddr, int tm) {
#pragma unroll
  for (int t = 0; t < 4; ++t)
    if (!MK || (tm >> t & 1)) {
      const u32x2 lo = trd(ta[t] + soff), hi = trd(ta[t] + soff + 2048);
      f.w[t] = u32x4{lo.x, lo.y, hi.x, hi.y};
    }
#pragma unroll
  for (int t = 0; t < 4; ++t) f.x[t] = lrd128(xaddr + t * 1024);
}
template <bool MK, int T0, int T1>
DEVI void mma_part(f32x4 (&acc)[4][4], const Frag& f, int tm) {
#pragma unroll
  for (int t = T0; t < T1; ++t)
    if (!MK || (tm >> t & 1)) {
#pragma unroll
      for (int m = 0; m < 4; ++m) acc[t][m] = mma16(f.w[t], f.x[m], acc[t][m]);
    }
}
DEVI int wrap18(int k) { return k >= 18 ? k - 18 : k; }
template <bool MK>
DEVI void gemm(f32x4 (&acc)[4][4], const unsigned (&ta)[4], unsigned xr0, unsigned xr1, unsigned slot0, const Strm& cur, int k0, int n,
               bool has_next, const Strm& nx, int nk0, int tm = 15) {
  Frag f0, f1;
  if (MK) {
#pragma unroll
    for (int t = 0; t < 4; ++t) f0.w[t] = f1.w[t] = u32x4{0u, 0u, 0u, 0u};
  }
  WAIT_V(4);
  read_frag<MK>(f0, ta, 0, xr0 + k0 * 4096, tm);
  WAIT_L0();
  int k = k0;
  issue_slab(slot0, cur, wrap18(k + 2));
  for (int it = 2; it < n; it += 2) {
    const int k2 = wrap18(k + 2);
    WAIT_V(4);
    read_frag<MK>(f1, ta, 4096, xr1 + k * 4096, tm);
    SCHED();
    mma_part<MK, 0, 2>(acc, f0, tm);
    SCHED();
    WAIT_L0();
    issue_slab(slot0 + 4096, cur, k2 + 1);
    SCHED();
    mma_part<MK, 2, 4>(acc, f0, tm);
    SCHED();
    WAIT_V(4);
    read_frag<MK>(f0, ta, 0, xr0 + k2 * 4096, tm);
    SCHED();
    mma_part<MK, 0, 2>(acc, f1, tm);
    SCHED();
    WAIT_L0();
    if (it + 2 < n) issue_slab(slot0, cur, wrap18(k2 + 2));
    else if (has_next) issue_slab(slot0, nx, nk0);
    SCHED();
    mma_part<MK, 2, 4>(acc, f1, tm);
    SCHED();
    k = k2;
  }
  if (has_next) WAIT_V(4); else WAIT_V(0);
  read_frag<MK>(f1, ta, 4096, xr1 + k * 4096, tm);
  SCHED();
  mma_part<MK, 0, 2>(acc, f0, tm);
  SCHED();
  WAIT_L0();
  if (has_next) issue_slab(slot0 + 4096, nx, nk0 + 1);
  SCHED();
  mma_part<MK, 2, 4>(acc, f0, tm);
  mma_part<MK, 0, 4>(acc, f1, tm);
}

DEVI float mish(float h) {
  float e = __builtin_amdgcn_exp2f(h * 1.4426950408889634f);
  float d = __builtin_fmaf(e, e + 2.0f, 2.0f);
  float r = __builtin_amdgcn_rcpf(d);
  return h * __builtin_fmaf(-2.0f, r, 1.0f);
}

DEVI void set_bias(f32x4 (&acc)[4][4], const u32x2 (&b)[4]) {
#pragma unroll
  for (int t = 0; t < 4; ++t)
#pragma unroll
    for (int m = 0; m < 4; ++m) acc[t][m] = f32x4{bflo(b[t].x), bfhi(b[t].x), bflo(b[t].y), bfhi(b[t].y)};
}
DEVI void set_zero(f32x4 (&acc)[4][4]) {
#pragma unroll
  for (int t = 0; t < 4; ++t)
#pragma unroll
    for (int m = 0; m < 4; ++m) acc[t][m] = f32x4{0.f, 0.f, 0.f, 0.f};
}
DEVI void mish_pack(const f32x4 (&acc)[4][4], u32x4 (&act)[2][4]) {
#pragma unroll
  for (int h = 0; h < 2; ++h)
#pragma unroll
    for (int m = 0; m < 4; ++m) {
      act[h][m] = u32x4{pk2(mish(acc[2 * h][m][0]), mish(acc[2 * h][m][1])), pk2(mish(acc[2 * h][m][2]), mish(acc[2 * h][m][3])),
                        pk2(mish(acc[2 * h + 1][m][0]), mish(acc[2 * h + 1][m][1])), pk2(mish(acc[2 * h + 1][m][2]), mish(acc[2 * h + 1][m][3]))};
      asm volatile("" : "+v"(act[h][m]));
    }
}
DEVI void write_act(const u32x4 (&act)[2][4], unsigned xr0, unsigned xr1, int blk) {
#pragma unroll
  for (int m = 0; m < 4; ++m) { lwr128(xr0 + blk * 8192 + m * 1024, act[0][m]); lwr128(xr1 + blk * 8192 + m * 1024, act[1][m]); }
}
DEVI void pack_part(const f32x4 (&acc)[4][4], u32x4 (&o)[8]) {
#pragma unroll
  for (int t = 0; t < 4; ++t)
#pragma unroll
    for (int h = 0; h < 2; ++h) {
      o[t * 2 + h] = u32x4{pk2(acc[t][2 * h][0], acc[t][2 * h][1]), pk2(acc[t][2 * h][2], acc[t][2 * h][3]),
                           pk2(acc[t][2 * h + 1][0], acc[t][2 * h + 1][1]), pk2(acc[t][2 * h + 1][2], acc[t][2 * h + 1][3])};
      asm volatile("" : "+v"(o[t * 2 + h]));
    }
}
DEVI void store_part(const u32x4 (&o)[8], char* P, int blk) {
#pragma unroll
  for (int e = 0; e < 8; ++e) *(u32x4*)(P + ((e & 1) * 36 + blk * 4 + (e >> 1)) * 1024) = o[e];
}

DEVI void hedgehog(const f32x16& p, unsigned (&out)[10]) {
  float mx = p[0], mn = p[0];
#pragma unroll
  for (int r = 1; r < 9; ++r) { mx = fmaxf(mx, p[r]); mn = fminf(mn, p[r]); }
  { const f32x2 a = halves(mx), b = halves(mn); mx = fmaxf(a.x, a.y); mn = fminf(b.x, b.y); }
  const float L2E = 1.4426950408889634f;
  const float mxl = mx * L2E, mnl = mn * L2E;
  float e[18], sp = 0.f, sn = 0.f;
#pragma unroll
  for (int r = 0; r < 9; ++r) {
    e[r] = __builtin_amdgcn_exp2f(__builtin_fmaf(p[r], L2E, -mxl));
    e[9 + r] = __builtin_amdgcn_exp2f(__builtin_fmaf(p[r], -L2E, mnl));
    sp += e[r]; sn += e[9 + r];
  }
  { const f32x2 a = halves(sp), b = halves(sn); sp = a.x + a.y; sn = b.x + b.y; }
  const float ip = __builtin_amdgcn_rcpf(sp), in = __builtin_amdgcn_rcpf(sn);
#pragma unroll
  for (int r = 0; r < 9; ++r) { e[r] *= ip; e[9 + r] *= in; }
#pragma unroll
  for (int r = 0; r < 9; ++r) out[r] = pk2(e[2 * r], e[2 * r + 1]);
  out[9] = 0u;
}

__global__ __launch_bounds__(512, 2) void k1(const u16* __restrict__ x, const u16* __restrict__ wqkv, const u16* __restrict__ wphi,
                                             const u16* __restrict__ wo, const u16* __restrict__ w1, const u16* __restrict__ b1,
                                             const u16* __restrict__ w2, char* __restrict__ ws) {
  __shared__ __attribute__((aligned(16))) char smem[LDS_BYTES];
  asm volatile("" ::"v"((unsigned)(uintptr_t)(LDS_AS char*)smem));
  const int tid = threadIdx.x, lane = tid & 63, w = __builtin_amdgcn_readfirstlane(tid >> 6);
  const int j = (blockIdx.x & 6) | ((blockIdx.x >> 3) & 1), i = (blockIdx.x >> 4) | ((blockIdx.x & 1) << 4);
  const int c16 = lane & 15, g = lane >> 4;
  const unsigned slot0 = RING_OFF + w * 8192;
  STAMP_DECL;
  STAMP(0);
  const int r8 = lane >> 3, sub = lane & 7;
  const int mrow = w * 8 + ((r8 >> 1) | ((r8 & 1) << 2));
  u32x4 xv[9];
  {
    const unsigned vo = mrow * 1152 + sub * 16;
    const char* xb = (const char*)x + (size_t)i * 64 * 1152;
#pragma unroll
    for (int c = 0; c < 9; ++c) asm volatile("global_load_dwordx4 %0, %1, %2 offset:%3" : "=v"(xv[c]) : "v"(vo), "s"(xb), "i"(c * 128) : "memory");
  }
  u32x2 bia[4], bi8[4];
  {
    const char* bb = (const char*)b1 + j * 1152;
    const unsigned vo = w * 128 + g * 8, v8 = 1024 + g * 8;
#pragma unroll
    for (int t = 0; t < 4; ++t) {
      asm volatile("global_load_dwordx2 %0, %1, %2 offset:%3" : "=v"(bia[t]) : "v"(vo), "s"(bb), "i"(t * 32) : "memory");
      asm volatile("global_load_dwordx2 %0, %1, %2 offset:%3" : "=v"(bi8[t]) : "v"(v8), "s"(bb), "i"(t * 32) : "memory");
    }
  }
  unsigned tagv;
  asm volatile("global_load_dword %0, %1, %2 sc1" : "=v"(tagv) : "v"(j * 128), "s"(ws + WS_EP) : "memory");
  asm volatile("s_barrier" ::: "memory");
  const int rot = 2 * (i % 9);
  const int part = (w - 1) >> 1, line = (w - 1) & 1;
  const Strm s1 = mk_strm((const char*)w1 + j * 1152 + w * 128, FF * 2, lane);
  const Strm sr = w == 0 ? mk_strm((const char*)w1 + j * 1152 + 1024, FF * 2, lane)
                         : mk_strm((const char*)wqkv + (part == 0 ? 2304 : (part - 1) * 1152) + (j + line) * 128, 3456, lane);
  if (w < 7) {
    issue_slab(slot0, sr, rot);
    issue_slab(slot0 + 4096, sr, rot + 1);
  } else {
#pragma unroll
    for (int s = 0; s < 3; ++s) { int c = s * 64 + lane; c = c > 161 ? 161 : c; dma16(WPHI_OFF + s * 1024, c * 16, (const char*)wphi + j * 2592); }
#pragma unroll
    for (int s = 0; s < 5; ++s) dma16(DUMMY_OFF, (j * 576 + (i & 15) * 36) * 1152 + (s * 64 + lane) * 128, (const char*)w2);
  }
  STAMP(1);
  asm volatile("s_waitcnt vmcnt(8)" : "+v"(xv[0]), "+v"(xv[1]), "+v"(xv[2]), "+v"(xv[3]), "+v"(xv[4]), "+v"(xv[5]), "+v"(xv[6]), "+v"(xv[7]), "+v"(xv[8]),
               "+v"(bia[0]), "+v"(bia[1]), "+v"(bia[2]), "+v"(bia[3]), "+v"(bi8[0]), "+v"(bi8[1]), "+v"(bi8[2]), "+v"(bi8[3]), "+v"(tagv)::"memory");
  const unsigned tag = __builtin_amdgcn_readfirstlane(tagv) + 1u;
  {
    STAMP(2);
    float s = 0.f, v = 0.f;
    const bf16x2 one2 = __builtin_bit_cast(bf16x2, 0x3f803f80u);
#pragma unroll
    for (int c = 0; c < 9; ++c)
#pragma unroll
      for (int e = 0; e < 4; ++e) {
        const unsigned xu = xv[c][e];
        const bf16x2 xx = __builtin_bit_cast(bf16x2, xu);
        s = __builtin_amdgcn_fdot2_f32_bf16(xx, one2, s, false);
        v = __builtin_amdgcn_fdot2_f32_bf16(xx, xx, v, false);
      }
    s += __shfl_xor(s, 1); v += __shfl_xor(v, 1); s += __shfl_xor(s, 2); v += __shfl_xor(v, 2); s += __shfl_xor(s, 4); v += __shfl_xor(v, 4);
    const float mean = s * (1.0f / 576.0f);
    const float var = fmaxf(__builtin_fmaf(-mean, mean, v * (1.0f / 576.0f)), 0.f);
    const float rs = __builtin_amdgcn_rsqf(var + 1e-5f), nb = -mean * rs;
    const int f = ((sub >> 2) << 1) + (sub & 1);
    const unsigned wa = XA_OFF + (sub >> 2) * 4096 + (mrow >> 4) * 1024 + ((sub & 1) * 32 + ((mrow & 15) ^ f)) * 16 + ((sub >> 1) & 1) * 8;
#pragma unroll
    for (int c = 0; c < 9; ++c) {
      unsigned o[4];
#pragma unroll
      for (int e = 0; e < 4; ++e) o[e] = pk2(__builtin_fmaf(bflo(xv[c][e]), rs, nb), __builtin_fmaf(bfhi(xv[c][e]), rs, nb));
      lwr64(wa + c * 8192, u32x2{o[0], o[1]});
      lwr64(wa + c * 8192 + 256, u32x2{o[2], o[3]});
    }
  }
  STAMP(3);
  BAR();
  STAMP(4);
  const unsigned xr0 = XA_OFF + (g * 16 + (c16 ^ (g >> 1))) * 16, xr1 = XA_OFF + 4096 + (g * 16 + (c16 ^ (2 + (g >> 1)))) * 16;
  unsigned ta[4];
  {
    const int q = (lane >> 2) & 3, p = lane & 3, r = 4 * g + q, s = (r >> 1) & 3;
#pragma unroll
    for (int t = 0; t < 4; ++t) ta[t] = slot0 + 128 * r + 32 * (t ^ s) + 8 * p;
  }
  f32x4 acc[4][4];
  if (w == 0) set_bias(acc, bi8); else set_zero(acc);
  if (w < 7) {
    const int tm = w == 0 ? 15 : (line == 0 ? (15 << (j >> 1)) & 15 : 15 >> (3 - (j >> 1)));
    gemm<true>(acc, ta, xr0, xr1, slot0, sr, rot, 18, w < 4, s1, rot, tm);
  } else {
    dma16(DUMMY_OFF, (j * 576 + (i & 15) * 36) * 1152 + (320 + (lane & 3)) * 128, (const char*)w2);
#pragma unroll
    for (int s = 0; s < 3; ++s) {
      const int fr = blockIdx.x + s * 256;
      if (fr < 648) {
        const int ks = fr / 36, nt = fr - ks * 36;
        const int ch = ks * 4 + g, c9 = ch % 9;
        const u16* src = wo + ((ch / 9) * 72 + (c9 < 8 ? 9 * c9 : 8)) * 576 + nt * 16 + c16;
        const int st = c9 < 8 ? 576 : 9 * 576;
        u32x4 o;
#pragma unroll
        for (int e = 0; e < 4; ++e) o[e] = (unsigned)src[(2 * e) * st] | ((unsigned)src[(2 * e + 1) * st] << 16);
        *(u32x4*)(ws + WS_WOT + ((size_t)fr * 64 + lane) * 16) = o;
      }
    }
    WAIT_V(0);
  }
  STAMP(5);
  BAR();
  if (w == 7) {
    lwr128(S2_OFF + lane * S2_ROW + 432, u32x4{0u, 0u, 0u, 0u});
    *(LDS_AS unsigned*)(uintptr_t)(WPHI_OFF + 3068) = 0u;
  } else if (w > 0) {
#pragma unroll
    for (int t = 0; t < 4; ++t) {
      const int lc = 64 * line + 16 * t + 4 * g - 8 * j;
      if (lc >= 0 && lc < 72) {
#pragma unroll
        for (int m = 0; m < 4; ++m)
          lwr64(S2_OFF + (m * 16 + c16) * S2_ROW + part * 144 + lc * 2, u32x2{pk2(acc[t][m][0], acc[t][m][1]), pk2(acc[t][m][2], acc[t][m][3])});
      }
    }
  }
  BAR();
  STAMP(6);
  const Strm s2 = mk_strm((const char*)w2 + (size_t)j * 576 * 1152 + w * 128, DM * 2, lane);
  char* P = ws + WS_P + ((size_t)(j * 64 + 2 * i) * 36 * 64 + lane) * 16;
  u32x4 act[2][4];
  if (w < 4) {
    u32x4 act8[2][4];
    if (w == 0) mish_pack(acc, act8);
    set_bias(acc, bia);
    gemm<false>(acc, ta, xr0, xr1, slot0, s1, rot, 18, true, s2, 16);
    STAMP(7);
    mish_pack(acc, act);
    if (w != 0) {
      const int col = i * 48 + (w - 1) * 16 + c16;
      unsigned pv[8];
      sweep<8>(ws + WS_KVP + ((size_t)(j * 32 + g * 8) * 1536 + (col < 1520 ? col : 0)) * 8, 1536 * 8, tag, pv);
      float a = 0.f, b = 0.f;
#pragma unroll
      for (int e = 0; e < 8; ++e) { a += bflo(pv[e]); b += bfhi(pv[e]); }
      a += __shfl_xor(a, 16); b += __shfl_xor(b, 16); a += __shfl_xor(a, 32); b += __shfl_xor(b, 32);
      if (g == 0) st_gran(ws + WS_KV + (size_t)(j * 1536 + col) * 8, tag, pk2(a, b));
      if (i == 0 && w == 1 && lane == 0) __hip_atomic_store((gu32*)(ws + WS_EP + j * 128), tag, __ATOMIC_RELAXED, __HIP_MEMORY_SCOPE_AGENT);
    }
    STAMP(8);
    BAR();
    write_act(act, xr0, xr1, w);
    if (w == 0) write_act(act8, xr0, xr1, 8);
    BAR();
    STAMP(9);
    set_zero(acc);
    gemm<false>(acc, ta, xr0, xr1, slot0, s2, 16, 10, true, s2, 8);
    STAMP(10);
    BAR();
    const Strm s8 = mk_strm((const char*)w2 + (size_t)j * 576 * 1152 + 1024, DM * 2, lane);
    gemm<false>(acc, ta, xr0, xr1, slot0, s2, 8, 8, true, s8, 4 * w);
    STAMP(11);
    u32x4 po[8], p8[8];
    pack_part(acc, po);
    set_zero(acc);
    gemm<false>(acc, ta, xr0, xr1, slot0, s8, 4 * w, w == 3 ? 6 : 4, false, s8, 0);
    store_part(po, P, w);
    pack_part(acc, p8);
#pragma unroll
    for (int e = 0; e < 8; ++e) lwr128(slot0 + e * 1024 + lane * 16, p8[e]);
    {
      const int r32 = lane & 31, hf = lane >> 5, reg = (r32 & 3) + 4 * (r32 >> 3);
      const int d = reg < 9 ? 9 * ((r32 >> 2) & 1) + reg : 18;
      unsigned kq[10];
      sweep<10>(ws + WS_KV + (size_t)(j * 1536 + w * 380 + d * 20 + hf * 10) * 8, 8, tag, kq);
      const int ch = 9 * j + 2 * w + hf, c8 = 9 * j + 8;
#pragma unroll
      for (int mh = 0; mh < 2; ++mh) {
        const unsigned pa = PH_OFF + w * 5120 + (mh * 32 + r32) * 80 + hf * 40;
        const u32x2 q0 = lrd64(pa), q1 = lrd64(pa + 8), q2 = lrd64(pa + 16), q3 = lrd64(pa + 24), q4 = lrd64(pa + 32);
        f32x16 o = {0.f, 0.f, 0.f, 0.f, 0.f, 0.f, 0.f, 0.f, 0.f, 0.f, 0.f, 0.f, 0.f, 0.f, 0.f, 0.f};
        o = mma32(u32x4{kq[0], kq[1], kq[2], kq[3]}, u32x4{q0.x, q0.y, q1.x, q1.y}, o);
        o = mma32(u32x4{kq[4], kq[5], kq[6], kq[7]}, u32x4{q2.x, q2.y, q3.x, q3.y}, o);
        o = mma32(u32x4{kq[8], kq[9], 0u, 0u}, u32x4{q4.x, q4.y, 0u, 0u}, o);
        const float inv = 1.0f / (o[9] + 1e-6f);
        char* ob = ws + WS_O + ((size_t)(2 * i + mh) * 36 + (r32 >> 4)) * 1024 + (r32 & 15) * 16;
        *(u32x4*)(ob + (ch >> 2) * 2048 + (ch & 3) * 256) =
            u32x4{pk2(o[0] * inv, o[1] * inv), pk2(o[2] * inv, o[3] * inv), pk2(o[4] * inv, o[5] * inv), pk2(o[6] * inv, o[7] * inv)};
        *(u16*)(ob + (c8 >> 2) * 2048 + (c8 & 3) * 256 + (2 * w + hf) * 2) = (u16)pk2(o[8] * inv, 0.f);
      }
    }
  } else {
    const int hh = w & 3, r32 = lane & 31, hf = lane >> 5;
    u32x4 wp[2];
    {
      const int reg = (r32 & 3) + 4 * (r32 >> 3), f = 9 * ((r32 >> 2) & 1) + reg;
#pragma unroll
      for (int s = 0; s < 2; ++s)
#pragma unroll
        for (int e = 0; e < 4; ++e) {
          unsigned v2[2];
#pragma unroll
          for (int b = 0; b < 2; ++b) {
            const int d = 16 * s + 8 * hf + 2 * e + b - 2 * hh;
            const bool ok = reg < 9 && d >= 0 && d < 18;
            const unsigned val = *(LDS_AS u16*)(uintptr_t)(WPHI_OFF + (ok ? (hh * 324 + d * 18 + f) * 2 : 0));
            v2[b] = ok ? val : 0u;
          }
          wp[s][e] = v2[0] | (v2[1] << 16);
        }
    }
    unsigned pq[2][10];
    const f32x16 z16 = {0.f, 0.f, 0.f, 0.f, 0.f, 0.f, 0.f, 0.f, 0.f, 0.f, 0.f, 0.f, 0.f, 0.f, 0.f, 0.f};
#pragma unroll
    for (int mh = 0; mh < 2; ++mh) {
      const unsigned rowa = S2_OFF + (mh * 32 + r32) * S2_ROW + hh * 32 + hf * 16;
      const f32x16 pk = mma32(wp[1], lrd128(rowa + 288 + 32), mma32(wp[0], lrd128(rowa + 288), z16));
      const f32x16 pqa = mma32(wp[1], lrd128(rowa + 144 + 32), mma32(wp[0], lrd128(rowa + 144), z16));
      unsigned ph[10];
      hedgehog(pk, ph);
#pragma unroll
      for (int e = 0; e < 5; ++e) lwr64(PH_OFF + hh * 5120 + (mh * 32 + r32) * 80 + hf * 40 + e * 8, u32x2{ph[2 * e], ph[2 * e + 1]});
      hedgehog(pqa, pq[mh]);
    }
    {
      const int q = (lane >> 2) & 3, p = lane & 3;
      const unsigned va = S2_OFF + (8 * g + q) * S2_ROW + 8 * p + hh * 32, pa = PH_OFF + hh * 5120 + (8 * g + q) * 80 + 8 * p;
      f32x4 kv[2][3];
#pragma unroll
      for (int e = 0; e < 6; ++e) kv[e / 3][e % 3] = f32x4{0.f, 0.f, 0.f, 0.f};
#pragma unroll
      for (int s = 0; s < 2; ++s) {
        u32x4 vf[2];
#pragma unroll
        for (int dt = 0; dt < 2; ++dt) {
          const u32x2 v0 = trd(va + s * 32 * S2_ROW + dt * 32), v1 = trd(va + (s * 32 + 4) * S2_ROW + dt * 32);
          vf[dt] = u32x4{v0.x, v0.y, v1.x, v1.y};
        }
        if (c16 == 15) vf[1] = u32x4{0x3f803f80u, 0x3f803f80u, 0x3f803f80u, 0x3f803f80u};
#pragma unroll
        for (int ft = 0; ft < 3; ++ft) {
          const u32x2 p0 = trd(pa + s * 32 * 80 + ft * 32), p1 = trd(pa + (s * 32 + 4) * 80 + ft * 32);
          const u32x4 pf = {p0.x, p0.y, p1.x, p1.y};
          kv[0][ft] = mma16(pf, vf[0], kv[0][ft]);
          kv[1][ft] = mma16(pf, vf[1], kv[1][ft]);
        }
      }
#pragma unroll
      for (int dt = 0; dt < 2; ++dt) {
        const int col = dt * 16 + c16, d = col == 31 ? 18 : col - 2 * hh;
        if (d >= 0 && (d < 18 || col == 31)) {
          char* dst = ws + WS_KVP + ((size_t)(j * 32 + i) * 1536 + hh * 380 + d * 20 + 2 * g) * 8;
#pragma unroll
          for (int ft = 0; ft < 3; ++ft)
            if (ft < 2 || g < 2) {
              st_gran(dst + ft * 64, tag, pk2(kv[dt][ft][0], kv[dt][ft][1]));
              st_gran(dst + ft * 64 + 8, tag, pk2(kv[dt][ft][2], kv[dt][ft][3]));
            }
        }
      }
#pragma unroll
      for (int mh = 0; mh < 2; ++mh)
#pragma unroll
        for (int e = 0; e < 5; ++e) lwr64(PH_OFF + hh * 5120 + (mh * 32 + r32) * 80 + hf * 40 + e * 8, u32x2{pq[mh][2 * e], pq[mh][2 * e + 1]});
    }
    WAIT_L0();
    {
      LDS_AS unsigned* cnt = (LDS_AS unsigned*)(uintptr_t)(WPHI_OFF + 3068);
      if (lane == 0) __hip_atomic_fetch_add(cnt, 1u, __ATOMIC_RELAXED, __HIP_MEMORY_SCOPE_WORKGROUP);
      while (__builtin_amdgcn_readfirstlane(__hip_atomic_load(cnt, __ATOMIC_RELAXED, __HIP_MEMORY_SCOPE_WORKGROUP)) < 4u) __builtin_amdgcn_s_sleep(1);
    }
    STAMP(7);
    issue_slab(slot0, s1, rot);
    issue_slab(slot0 + 4096, s1, rot + 1);
    set_bias(acc, bia);
    gemm<false>(acc, ta, xr0, xr1, slot0, s1, rot, 18, true, s2, 0);
    STAMP(8);
    BAR();
    BAR();
    STAMP(9);
    mish_pack(acc, act);
    write_act(act, xr0, xr1, w);
    STAMP(10);
    BAR();
    set_zero(acc);
    gemm<false>(acc, ta, xr0, xr1, slot0, s2, 0, 18, false, s2, 0);
    STAMP(11);
    u32x4 po[8];
    pack_part(acc, po);
    store_part(po, P, w);
  }
  STAMP(12);
  BAR();
  {
    float v[8] = {0.f, 0.f, 0.f, 0.f, 0.f, 0.f, 0.f, 0.f};
#pragma unroll
    for (int a = 0; a < 4; ++a) {
      const u32x4 u = lrd128(RING_OFF + a * 8192 + w * 1024 + lane * 16);
#pragma unroll
      for (int e = 0; e < 4; ++e) { v[2 * e] += bflo(u[e]); v[2 * e + 1] += bfhi(u[e]); }
    }
    *(u32x4*)(P + ((w & 1) * 36 + 32 + (w >> 1)) * 1024) = u32x4{pk2(v[0], v[1]), pk2(v[2], v[3]), pk2(v[4], v[5]), pk2(v[6], v[7])};
  }
  STAMP(13);
  STAMP(14); STAMP(15);
  STAMP_FLUSH(ws);
}

__global__ __launch_bounds__(576) void k2(const u16* __restrict__ x, const char* __restrict__ ws, u16* __restrict__ out) {
  __shared__ __attribute__((aligned(16))) char ob[36864];
  const int tid = threadIdx.x, lane = tid & 63, w = __builtin_amdgcn_readfirstlane(tid >> 6);
  const int b = blockIdx.x, i32 = (b & 7) + 8 * (b >> 5), nt = ((b >> 3) & 3) * 9 + w;
  const int c16 = lane & 15, g = lane >> 4;
  const unsigned obase = (unsigned)(uintptr_t)(LDS_AS char*)ob;
#pragma unroll
  for (int s = 0; s < 4; ++s) dma16(obase + (w * 4 + s) * 1024, lane * 16, ws + WS_O + ((size_t)i32 * 36 + w * 4 + s) * 1024);
  u32x4 wf[18], pp[8];
  u32x2 xr[2];
#pragma unroll
  for (int ks = 0; ks < 18; ++ks) wf[ks] = *(const u32x4*)(ws + WS_WOT + ((size_t)(ks * 36 + nt) * 64 + lane) * 16);
#pragma unroll
  for (int s = 0; s < 8; ++s) pp[s] = *(const u32x4*)(ws + WS_P + ((size_t)((s * 64 + i32) * 36 + nt) * 64 + lane) * 16);
#pragma unroll
  for (int m = 0; m < 2; ++m) xr[m] = *(const u32x2*)(x + (size_t)(i32 * 32 + m * 16 + c16) * 576 + nt * 16 + g * 4);
  WAIT_V(28);
  BAR();
  f32x4 acc[2] = {{0.f, 0.f, 0.f, 0.f}, {0.f, 0.f, 0.f, 0.f}};
#pragma unroll
  for (int ks = 0; ks < 18; ++ks)
#pragma unroll
    for (int m = 0; m < 2; ++m) acc[m] = mma16(wf[ks], lrd128(obase + (ks * 2 + m) * 1024 + lane * 16), acc[m]);
#pragma unroll
  for (int m = 0; m < 2; ++m) {
    float bs[4] = {0.f, 0.f, 0.f, 0.f};
#pragma unroll
    for (int s = 0; s < 8; ++s) {
      bs[0] += bflo(pp[s][2 * m]); bs[1] += bfhi(pp[s][2 * m]); bs[2] += bflo(pp[s][2 * m + 1]); bs[3] += bfhi(pp[s][2 * m + 1]);
    }
    const float xs[4] = {bflo(xr[m].x), bfhi(xr[m].x), bflo(xr[m].y), bfhi(xr[m].y)};
    float o[4];
#pragma unroll
    for (int r = 0; r < 4; ++r) {
      const float sg = __builtin_amdgcn_rcpf(1.0f + __builtin_amdgcn_exp2f(-1.4426950408889634f * bs[r]));
      o[r] = __builtin_fmaf(acc[m][r], bs[r] * sg, xs[r]);
    }
    *(u32x2*)(out + (size_t)(i32 * 32 + m * 16 + c16) * 576 + nt * 16 + g * 4) = u32x2{pk2(o[0], o[1]), pk2(o[2], o[3])};
  }
}

extern "C" void kernel_launch(void* const* d_in, const int* in_sizes, int n_in, void* d_out, int out_size, void* d_ws, size_t ws_size,
                              hipStream_t stream) {
  const u16* x = (const u16*)d_in[0];
  const u16* wqkv = (const u16*)d_in[1];
  const u16* wphi = (const u16*)d_in[2];
  const u16* wo = (const u16*)d_in[3];
  const u16* w1 = (const u16*)d_in[5];
  const u16* b1 = (const u16*)d_in[6];
  const u16* w2 = (const u16*)d_in[7];
  char* ws = (char*)d_ws;
  k1<<<256, 512, 0, stream>>>(x, wqkv, wphi, wo, w1, b1, w2, ws);
  k2<<<256, 576, 0, stream>>>(x, ws, (u16*)d_out);
}
```

```cpp
#include <hip/hip_runtime.h>
#include <hip/hip_bf16.h>
#include <stdint.h>

typedef __attribute__((ext_vector_type(8))) __bf16 bf16x8;
typedef __attribute__((ext_vector_type(2))) __bf16 bf16x2;
typedef __attribute__((ext_vector_type(4))) short s16x4;
typedef __attribute__((ext_vector_type(2))) float f32x2;
typedef __attribute__((ext_vector_type(4))) float f32x4;
typedef __attribute__((ext_vector_type(16))) float f32x16;
typedef __attribute__((ext_vector_type(4))) unsigned u32x4;
typedef __attribute__((ext_vector_type(2))) unsigned u32x2;
typedef unsigned short u16;
#define LDS_AS __attribute__((address_space(3)))
#define DEVI __device__ __forceinline__

constexpr int SEQ = 2048, DM = 576, FF = 4608;
constexpr int XA_OFF = 0;
constexpr int RING_OFF = 73728;
constexpr int S2_OFF = RING_OFF + 32768;
constexpr int S2_ROW = 464;
constexpr int WPHI_OFF = RING_OFF + 65536;
constexpr int PH_OFF = WPHI_OFF + 3072;
constexpr int DUMMY_OFF = PH_OFF + 20480;
constexpr int LDS_BYTES = 163840;
constexpr size_t WS_P = 0;
constexpr size_t WS_KVP = WS_P + (size_t)8 * 64 * 36 * 1024;
constexpr size_t WS_KV = WS_KVP + (size_t)8 * 32 * 1536 * 8;
constexpr size_t WS_EP = WS_KV + (size_t)8 * 1536 * 8;
constexpr size_t WS_O = WS_EP + 1024;
constexpr size_t WS_WOT = WS_O + (size_t)64 * 36 * 1024;

#define STAMP(n)
#define STAMP_DECL
#define STAMP_ARG
#define STAMP_PASS
#define STAMP_FLUSH(ws)
DEVI unsigned pk2(float a, float b) {
  f32x2 v = {a, b};
  return __builtin_bit_cast(unsigned, __builtin_convertvector(v, bf16x2));
}
DEVI float bflo(unsigned u) { return __builtin_bit_cast(float, u << 16); }
DEVI float bfhi(unsigned u) { return __builtin_bit_cast(float, u & 0xffff0000u); }
DEVI u32x2 trd(unsigned a) {
  return __builtin_bit_cast(u32x2, __builtin_amdgcn_ds_read_tr16_b64_v4i16((LDS_AS s16x4*)(uintptr_t)a));
}
DEVI u32x4 lrd128(unsigned a) { return *(LDS_AS u32x4*)(uintptr_t)a; }
DEVI u32x2 lrd64(unsigned a) { return *(LDS_AS u32x2*)(uintptr_t)a; }
DEVI void lwr128(unsigned a, u32x4 v) { *(LDS_AS u32x4*)(uintptr_t)a = v; }
DEVI void lwr64(unsigned a, u32x2 v) { *(LDS_AS u32x2*)(uintptr_t)a = v; }
DEVI f32x4 mma16(u32x4 a, u32x4 b, f32x4 c) {
  return __builtin_amdgcn_mfma_f32_16x16x32_bf16(__builtin_bit_cast(bf16x8, a), __builtin_bit_cast(bf16x8, b), c, 0, 0, 0);
}
DEVI f32x16 mma32(u32x4 a, u32x4 b, f32x16 c) {
  return __builtin_amdgcn_mfma_f32_32x32x16_bf16(__builtin_bit_cast(bf16x8, a), __builtin_bit_cast(bf16x8, b), c, 0, 0, 0);
}
DEVI void dma16(unsigned ldsb, unsigned voff, const char* base) {
  asm volatile("s_mov_b32 m0, %0\n\ts_nop 0\n\tglobal_load_lds_dwordx4 %1, %2" ::"s"(ldsb), "v"(voff), "s"(base) : "memory");
}
#define WAIT_V(n) asm volatile("s_waitcnt vmcnt(%0)" ::"i"(n) : "memory")
#define WAIT_L0() do { asm volatile("" ::: "memory"); __builtin_amdgcn_s_waitcnt(0xC07F); asm volatile("" ::: "memory"); } while (0)
#define BAR() asm volatile("s_waitcnt lgkmcnt(0)\n\ts_barrier" ::: "memory")
typedef __attribute__((address_space(1))) unsigned long long gu64;
typedef __attribute__((address_space(1))) unsigned gu32;
DEVI void st_gran(char* p, unsigned tag, unsigned v) {
  __hip_atomic_store((gu64*)p, ((unsigned long long)tag << 32) | v, __ATOMIC_RELAXED, __HIP_MEMORY_SCOPE_AGENT);
}
template <int N>
DEVI void sweep(const char* p, int S, unsigned tag, unsigned (&v)[N]) {
#pragma nounroll
  for (int spins = 0; spins < (1 << 16); ++spins) {
    bool ok = true;
#pragma unroll
    for (int k = 0; k < N; ++k) {
      const unsigned long long x = __hip_atomic_load((gu64*)(p + k * S), __ATOMIC_RELAXED, __HIP_MEMORY_SCOPE_AGENT);
      v[k] = (unsigned)x;
      ok &= (unsigned)(x >> 32) == tag;
    }
    if (__all(ok)) break;
    __builtin_amdgcn_s_sleep(8);
  }
}
#define SCHED() __builtin_amdgcn_sched_barrier(0)
DEVI f32x2 halves(float v) {
  const unsigned u = __builtin_bit_cast(unsigned, v);
  auto r = __builtin_amdgcn_permlane32_swap(u, u, false, false);
  const unsigned r0 = r[0], r1 = r[1];
  return f32x2{__builtin_bit_cast(float, r0), __builtin_bit_cast(float, r1)};
}

DEVI void issue_slab(unsigned slot, const char* base, int ld, int ks, unsigned r8, unsigned chs) {
  if (ld == DM * 2 && ks < 16) ks = ((ks & 6) << 1) | ((ks >> 3) << 1) | (ks & 1);
  const unsigned voff = __umul24(r8, ld) + chs;
  const char* rb = base + (size_t)(ks * 32 * ld);
#pragma unroll
  for (int i = 0; i < 4; ++i) dma16(slot + i * 1024, voff, rb + (size_t)(i * 8 * ld));
}
struct Frag { u32x4 w[4], x[4]; };
DEVI void read_frag(Frag& f, const unsigned (&ta)[4], unsigned soff, unsigned xaddr) {
#pragma unroll
  for (int t = 0; t < 4; ++t) {
    const u32x2 lo = trd(ta[t] + soff), hi = trd(ta[t] + soff + 2048);
    f.w[t] = u32x4{lo.x, lo.y, hi.x, hi.y};
  }
#pragma unroll
  for (int t = 0; t < 4; ++t) f.x[t] = lrd128(xaddr + t * 1024);
}
template <int T0, int T1>
DEVI void mma_part(f32x4 (&acc)[4][4], const Frag& f, int tm) {
#pragma unroll
  for (int t = T0; t < T1; ++t)
    if (tm >> t & 1) {
#pragma unroll
      for (int m = 0; m < 4; ++m) acc[t][m] = mma16(f.w[t], f.x[m], acc[t][m]);
    }
}
DEVI int wrap18(int k) { return k >= 18 ? k - 18 : k; }
DEVI void gemm(f32x4 (&acc)[4][4], const unsigned (&ta)[4], unsigned xr0, unsigned xr1, unsigned slot0, unsigned r8, unsigned chs,
               const char* cb, int cld, int k0, int n, const char* nb, int nld, int nk0, int tm) {
  Frag f0, f1;
  WAIT_V(4);
  read_frag(f0, ta, 0, xr0 + k0 * 4096);
  WAIT_L0();
  int k = k0;
  issue_slab(slot0, cb, cld, wrap18(k + 2), r8, chs);
#pragma nounroll
  for (int left = n - 2; left >= 0; left -= 2) {
    const int k2 = wrap18(k + 2);
    if (left > 0 || nld) WAIT_V(4); else WAIT_V(0);
    read_frag(f1, ta, 4096, xr1 + k * 4096);
    SCHED();
    mma_part<0, 2>(acc, f0, tm);
    SCHED();
    WAIT_L0();
    if (left > 0 || nld) issue_slab(slot0 + 4096, left > 0 ? cb : nb, left > 0 ? cld : nld, (left > 0 ? k2 : nk0) + 1, r8, chs);
    SCHED();
    mma_part<2, 4>(acc, f0, tm);
    SCHED();
    if (left > 0) {
      WAIT_V(4);
      read_frag(f0, ta, 0, xr0 + k2 * 4096);
    }
    SCHED();
    mma_part<0, 2>(acc, f1, tm);
    SCHED();
    WAIT_L0();
    if (left > 2 || (left == 2 && nld)) issue_slab(slot0, left > 2 ? cb : nb, left > 2 ? cld : nld, left > 2 ? wrap18(k2 + 2) : nk0, r8, chs);
    SCHED();
    mma_part<2, 4>(acc, f1, tm);
    SCHED();
    k = k2;
  }
}

DEVI float mish(float h) {
  float e = __builtin_amdgcn_exp2f(h * 1.4426950408889634f);
  float d = __builtin_fmaf(e, e + 2.0f, 2.0f);
  float r = __builtin_amdgcn_rcpf(d);
  return h * __builtin_fmaf(-2.0f, r, 1.0f);
}

DEVI void set_bias(f32x4 (&acc)[4][4], const u32x2 (&b)[4]) {
#pragma unroll
  for (int t = 0; t < 4; ++t)
#pragma unroll
    for (int m = 0; m < 4; ++m) acc[t][m] = f32x4{bflo(b[t].x), bfhi(b[t].x), bflo(b[t].y), bfhi(b[t].y)};
}
DEVI void set_zero(f32x4 (&acc)[4][4]) {
#pragma unroll
  for (int t = 0; t < 4; ++t)
#pragma unroll
    for (int m = 0; m < 4; ++m) acc[t][m] = f32x4{0.f, 0.f, 0.f, 0.f};
}
DEVI void mish_pack(const f32x4 (&acc)[4][4], u32x4 (&act)[2][4]) {
#pragma unroll
  for (int h = 0; h < 2; ++h)
#pragma unroll
    for (int m = 0; m < 4; ++m) {
      act[h][m] = u32x4{pk2(mish(acc[2 * h][m][0]), mish(acc[2 * h][m][1])), pk2(mish(acc[2 * h][m][2]), mish(acc[2 * h][m][3])),
                        pk2(mish(acc[2 * h + 1][m][0]), mish(acc[2 * h + 1][m][1])), pk2(mish(acc[2 * h + 1][m][2]), mish(acc[2 * h + 1][m][3]))};
      asm volatile("" : "+v"(act[h][m]));
    }
}
DEVI void write_act(const u32x4 (&act)[2][4], unsigned xr0, unsigned xr1, int blk) {
#pragma unroll
  for (int m = 0; m < 4; ++m) { lwr128(xr0 + blk * 8192 + m * 1024, act[0][m]); lwr128(xr1 + blk * 8192 + m * 1024, act[1][m]); }
}
DEVI void pack_part(const f32x4 (&acc)[4][4], u32x4 (&o)[8]) {
#pragma unroll
  for (int t = 0; t < 4; ++t)
#pragma unroll
    for (int h = 0; h < 2; ++h) {
      o[t * 2 + h] = u32x4{pk2(acc[t][2 * h][0], acc[t][2 * h][1]), pk2(acc[t][2 * h][2], acc[t][2 * h][3]),
                           pk2(acc[t][2 * h + 1][0], acc[t][2 * h + 1][1]), pk2(acc[t][2 * h + 1][2], acc[t][2 * h + 1][3])};
      asm volatile("" : "+v"(o[t * 2 + h]));
    }
}
DEVI void store_part(const u32x4 (&o)[8], char* P, int blk) {
#pragma unroll
  for (int e = 0; e < 8; ++e) *(u32x4*)(P + ((e & 1) * 36 + blk * 4 + (e >> 1)) * 1024) = o[e];
}

DEVI void hedgehog(const f32x16& p, unsigned (&out)[10]) {
  float mx = p[0], mn = p[0];
#pragma unroll
  for (int r = 1; r < 9; ++r) { mx = fmaxf(mx, p[r]); mn = fminf(mn, p[r]); }
  { const f32x2 a = halves(mx), b = halves(mn); mx = fmaxf(a.x, a.y); mn = fminf(b.x, b.y); }
  const float L2E = 1.4426950408889634f;
  const float mxl = mx * L2E, mnl = mn * L2E;
  float e[18], sp = 0.f, sn = 0.f;
#pragma unroll
  for (int r = 0; r < 9; ++r) {
    e[r] = __builtin_amdgcn_exp2f(__builtin_fmaf(p[r], L2E, -mxl));
    e[9 + r] = __builtin_amdgcn_exp2f(__builtin_fmaf(p[r], -L2E, mnl));
    sp += e[r]; sn += e[9 + r];
  }
  { const f32x2 a = halves(sp), b = halves(sn); sp = a.x + a.y; sn = b.x + b.y; }
  const float ip = __builtin_amdgcn_rcpf(sp), in = __builtin_amdgcn_rcpf(sn);
#pragma unroll
  for (int r = 0; r < 9; ++r) { e[r] *= ip; e[9 + r] *= in; }
#pragma unroll
  for (int r = 0; r < 9; ++r) out[r] = pk2(e[2 * r], e[2 * r + 1]);
  out[9] = 0u;
}

__global__ __launch_bounds__(512, 2) void k1(const u16* __restrict__ x, const u16* __restrict__ wqkv, const u16* __restrict__ wphi,
                                             const u16* __restrict__ wo, const u16* __restrict__ w1, const u16* __restrict__ b1,
                                             const u16* __restrict__ w2, char* __restrict__ ws) {
  __shared__ __attribute__((aligned(16))) char smem[LDS_BYTES];
  asm volatile("" ::"v"((unsigned)(uintptr_t)(LDS_AS char*)smem));
  const int tid = threadIdx.x, lane = tid & 63, w = __builtin_amdgcn_readfirstlane(tid >> 6);
  const int j = (blockIdx.x & 6) | ((blockIdx.x >> 3) & 1), i = (blockIdx.x >> 4) | ((blockIdx.x & 1) << 4);
  const int c16 = lane & 15, g = lane >> 4;
  const unsigned slot0 = RING_OFF + w * 8192;
  STAMP_DECL;
  STAMP(0);
  const int r8 = lane >> 3, sub = lane & 7;
  const int mrow = w * 8 + ((r8 >> 1) | ((r8 & 1) << 2));
  u32x4 xv[9];
  {
    const unsigned vo = mrow * 1152 + sub * 16;
    const char* xb = (const char*)x + (size_t)i * 64 * 1152;
#pragma unroll
    for (int c = 0; c < 9; ++c) asm volatile("global_load_dwordx4 %0, %1, %2 offset:%3" : "=v"(xv[c]) : "v"(vo), "s"(xb), "i"(c * 128) : "memory");
  }
  const int fb = ((w & 3) << 1) | (w >> 2);
  u32x2 bia[4], bi8[4];
  {
    const char* bb = (const char*)b1 + j * 1152;
    const unsigned vo = fb * 128 + g * 8, v8 = 1024 + g * 8;
#pragma unroll
    for (int t = 0; t < 4; ++t) {
      asm volatile("global_load_dwordx2 %0, %1, %2 offset:%3" : "=v"(bia[t]) : "v"(vo), "s"(bb), "i"(t * 32) : "memory");
      asm volatile("global_load_dwordx2 %0, %1, %2 offset:%3" : "=v"(bi8[t]) : "v"(v8), "s"(bb), "i"(t * 32) : "memory");
    }
  }
  unsigned tagv;
  asm volatile("global_load_dword %0, %1, %2 sc1" : "=v"(tagv) : "v"(j * 128), "s"(ws + WS_EP) : "memory");
  asm volatile("s_barrier" ::: "memory");
  const int rot = 2 * (i % 9);
  const int part = (w - 1) >> 1, line = (w - 1) & 1;
  const unsigned chs = ((lane & 7) ^ (((lane >> 4) & 3) << 1)) * 16;
  const char* const s1 = (const char*)w1 + j * 1152 + fb * 128;
  const char* const s2 = (const char*)w2 + (size_t)j * 576 * 1152 + w * 128;
  const char* const s8 = (const char*)w2 + (size_t)j * 576 * 1152 + 1024;
  const char* const sr = w == 0 ? (const char*)w1 + j * 1152 + 1024 : (const char*)wqkv + (part == 0 ? 2304 : (part - 1) * 1152) + (j + line) * 128;
  const int srl = w == 0 ? FF * 2 : 3456;
  if (w < 7) {
#pragma nounroll
    for (int s = 0; s < 2; ++s) issue_slab(slot0 + s * 4096, sr, srl, rot + s, r8, chs);
  } else {
#pragma unroll
    for (int s = 0; s < 3; ++s) { int c = s * 64 + lane; c = c > 161 ? 161 : c; dma16(WPHI_OFF + s * 1024, c * 16, (const char*)wphi + j * 2592); }
#pragma unroll
    for (int s = 0; s < 5; ++s) dma16(DUMMY_OFF, (j * 576 + (i & 15) * 36) * 1152 + (s * 64 + lane) * 128, (const char*)w2);
  }
  STAMP(1);
  asm volatile("s_waitcnt vmcnt(8)" : "+v"(xv[0]), "+v"(xv[1]), "+v"(xv[2]), "+v"(xv[3]), "+v"(xv[4]), "+v"(xv[5]), "+v"(xv[6]), "+v"(xv[7]), "+v"(xv[8]),
               "+v"(bia[0]), "+v"(bia[1]), "+v"(bia[2]), "+v"(bia[3]), "+v"(bi8[0]), "+v"(bi8[1]), "+v"(bi8[2]), "+v"(bi8[3]), "+v"(tagv)::"memory");
  const unsigned tag = __builtin_amdgcn_readfirstlane(tagv) + 1u;
  {
    STAMP(2);
    float s = 0.f, v = 0.f;
    const bf16x2 one2 = __builtin_bit_cast(bf16x2, 0x3f803f80u);
#pragma unroll
    for (int c = 0; c < 9; ++c)
#pragma unroll
      for (int e = 0; e < 4; ++e) {
        const unsigned xu = xv[c][e];
        const bf16x2 xx = __builtin_bit_cast(bf16x2, xu);
        s = __builtin_amdgcn_fdot2_f32_bf16(xx, one2, s, false);
        v = __builtin_amdgcn_fdot2_f32_bf16(xx, xx, v, false);
      }
    s += __shfl_xor(s, 1); v += __shfl_xor(v, 1); s += __shfl_xor(s, 2); v += __shfl_xor(v, 2); s += __shfl_xor(s, 4); v += __shfl_xor(v, 4);
    const float mean = s * (1.0f / 576.0f);
    const float var = fmaxf(__builtin_fmaf(-mean, mean, v * (1.0f / 576.0f)), 0.f);
    const float rs = __builtin_amdgcn_rsqf(var + 1e-5f), nb = -mean * rs;
    const int f = ((sub >> 2) << 1) + (sub & 1);
    const unsigned wa = XA_OFF + (sub >> 2) * 4096 + (mrow >> 4) * 1024 + ((sub & 1) * 32 + ((mrow & 15) ^ f)) * 16 + ((sub >> 1) & 1) * 8;
#pragma unroll
    for (int c = 0; c < 9; ++c) {
      unsigned o[4];
#pragma unroll
      for (int e = 0; e < 4; ++e) o[e] = pk2(__builtin_fmaf(bflo(xv[c][e]), rs, nb), __builtin_fmaf(bfhi(xv[c][e]), rs, nb));
      lwr64(wa + c * 8192, u32x2{o[0], o[1]});
      lwr64(wa + c * 8192 + 256, u32x2{o[2], o[3]});
    }
  }
  STAMP(3);
  BAR();
  STAMP(4);
  const unsigned xr0 = XA_OFF + (g * 16 + (c16 ^ (g >> 1))) * 16, xr1 = XA_OFF + 4096 + (g * 16 + (c16 ^ (2 + (g >> 1)))) * 16;
  unsigned ta[4];
  {
    const int q = (lane >> 2) & 3, p = lane & 3, r = 4 * g + q, s = (r >> 1) & 3;
#pragma unroll
    for (int t = 0; t < 4; ++t) ta[t] = slot0 + 128 * r + 32 * (t ^ s) + 8 * p;
  }
  f32x4 acc[4][4];
  u32x4 keep[8];
  char* const Pb = ws + WS_P + (size_t)(j * 64 + 2 * i) * 36 * 64 * 16;
  const bool grpA = w < 4;
  if (w != 0) {
#pragma unroll
    for (int t = 0; t < 4; ++t) bi8[t] = u32x2{0u, 0u};
  }
  set_bias(acc, bi8);
  if (w == 7) {
    dma16(DUMMY_OFF, (j * 576 + (i & 15) * 36) * 1152 + (320 + (lane & 3)) * 128, (const char*)w2);
#pragma nounroll
    for (int s = 0; s < 3; ++s) {
      const int fr = blockIdx.x + s * 256;
      if (fr < 648) {
        const int ks = fr / 36, nt = fr - ks * 36;
        const int ch = ks * 4 + g, c9 = ch % 9;
        const u16* src = wo + ((ch / 9) * 72 + (c9 < 8 ? 9 * c9 : 8)) * 576 + nt * 16 + c16;
        const int st = c9 < 8 ? 576 : 9 * 576;
        u32x4 o;
#pragma unroll
        for (int e = 0; e < 4; ++e) o[e] = (unsigned)src[(2 * e) * st] | ((unsigned)src[(2 * e + 1) * st] << 16);
        *(u32x4*)(ws + WS_WOT + ((size_t)fr * 64 + lane) * 16) = o;
      }
    }
    WAIT_V(0);
  }
#pragma nounroll
  for (int ph = 0; ph < (grpA ? 5 : 3); ++ph) {
    const char* cb; const char* nb;
    int cld, k0, n, nld, nk0, tm = 15;
    if (ph == 0) {
      cb = sr; cld = srl; k0 = rot; n = 18; nb = s1; nld = grpA ? FF * 2 : 0; nk0 = rot;
      if (w != 0) tm = line == 0 ? (15 << (j >> 1)) & 15 : 15 >> (3 - (j >> 1));
    } else if (ph == 1) { cb = s1; cld = FF * 2; k0 = rot; n = 18; nb = s2; nld = DM * 2; nk0 = grpA ? 16 : 0; }
    else if (ph == 2) { cb = s2; cld = DM * 2; k0 = grpA ? 16 : 0; n = grpA ? 10 : 18; nb = s2; nld = grpA ? DM * 2 : 0; nk0 = 8; }
    else if (ph == 3) { cb = s2; cld = DM * 2; k0 = 8; n = 8; nb = s8; nld = DM * 2; nk0 = 4 * w; }
    else { cb = s8; cld = DM * 2; k0 = 4 * w; n = w == 3 ? 6 : 4; nb = s8; nld = 0; nk0 = 0; }
    if (ph > 0 || w < 7) gemm(acc, ta, xr0, xr1, slot0, r8, chs, cb, cld, k0, n, nb, nld, nk0, tm);
    int ln = tid & 63;
    asm volatile("" : "+v"(ln));
    const int lane = ln, c16 = ln & 15, g = ln >> 4;
    u32x4 act[2][4];
    if (ph == 0) {
      BAR();
  if (w == 7) {
    lwr128(S2_OFF + lane * S2_ROW + 432, u32x4{0u, 0u, 0u, 0u});
    *(LDS_AS unsigned*)(uintptr_t)(WPHI_OFF + 3068) = 0u;
  } else if (w > 0) {
#pragma unroll
    for (int t = 0; t < 4; ++t) {
      const int lc = 64 * line + 16 * t + 4 * g - 8 * j;
      if (lc >= 0 && lc < 72) {
#pragma unroll
        for (int m = 0; m < 4; ++m)
          lwr64(S2_OFF + (m * 16 + c16) * S2_ROW + part * 144 + lc * 2, u32x2{pk2(acc[t][m][0], acc[t][m][1]), pk2(acc[t][m][2], acc[t][m][3])});
      }
    }
  }
      BAR();
      STAMP(6);
    } else if (ph == 1 && !grpA) {
      BAR();
      BAR();
    }
    if (ph == 1 || (ph == 0 && w == 0)) mish_pack(acc, act);
    if (ph == 0) {
      if (w == 0) {
#pragma unroll
        for (int e = 0; e < 8; ++e) keep[e] = act[e >> 2][e & 3];
      } else if (!grpA) {
    const int hh = w & 3, r32 = lane & 31, hf = lane >> 5;
    u32x4 wp[2];
    {
      const int reg = (r32 & 3) + 4 * (r32 >> 3), f = 9 * ((r32 >> 2) & 1) + reg;
#pragma unroll
      for (int s = 0; s < 2; ++s)
#pragma unroll
        for (int e = 0; e < 4; ++e) {
          unsigned v2[2];
#pragma unroll
          for (int b = 0; b < 2; ++b) {
            const int d = 16 * s + 8 * hf + 2 * e + b - 2 * hh;
            const bool ok = reg < 9 && d >= 0 && d < 18;
            const unsigned val = *(LDS_AS u16*)(uintptr_t)(WPHI_OFF + (ok ? (hh * 324 + d * 18 + f) * 2 : 0));
            v2[b] = ok ? val : 0u;
          }
          wp[s][e] = v2[0] | (v2[1] << 16);
        }
    }
#pragma nounroll
        for (int u = 0; u < 4; ++u) {
          if (u == 2) {
      const int q = (lane >> 2) & 3, p = lane & 3;
      const unsigned va = S2_OFF + (8 * g + q) * S2_ROW + 8 * p + hh * 32, pa = PH_OFF + hh * 5120 + (8 * g + q) * 80 + 8 * p;
      f32x4 kv[2][3];
#pragma unroll
      for (int e = 0; e < 6; ++e) kv[e / 3][e % 3] = f32x4{0.f, 0.f, 0.f, 0.f};
#pragma unroll
      for (int s = 0; s < 2; ++s) {
        u32x4 vf[2];
#pragma unroll
        for (int dt = 0; dt < 2; ++dt) {
          const u32x2 v0 = trd(va + s * 32 * S2_ROW + dt * 32), v1 = trd(va + (s * 32 + 4) * S2_ROW + dt * 32);
          vf[dt] = u32x4{v0.x, v0.y, v1.x, v1.y};
        }
        if (c16 == 15) vf[1] = u32x4{0x3f803f80u, 0x3f803f80u, 0x3f803f80u, 0x3f803f80u};
#pragma unroll
        for (int ft = 0; ft < 3; ++ft) {
          const u32x2 p0 = trd(pa + s * 32 * 80 + ft * 32), p1 = trd(pa + (s * 32 + 4) * 80 + ft * 32);
          const u32x4 pf = {p0.x, p0.y, p1.x, p1.y};
          kv[0][ft] = mma16(pf, vf[0], kv[0][ft]);
          kv[1][ft] = mma16(pf, vf[1], kv[1][ft]);
        }
      }
#pragma unroll
      for (int dt = 0; dt < 2; ++dt) {
        const int col = dt * 16 + c16, d = col == 31 ? 18 : col - 2 * hh;
        if (d >= 0 && (d < 18 || col == 31)) {
          char* dst = ws + WS_KVP + ((size_t)(j * 32 + i) * 1536 + hh * 380 + d * 20 + 2 * g) * 8;
#pragma unroll
          for (int ft = 0; ft < 3; ++ft)
            if (ft < 2 || g < 2) {
              st_gran(dst + ft * 64, tag, pk2(kv[dt][ft][0], kv[dt][ft][1]));
              st_gran(dst + ft * 64 + 8, tag, pk2(kv[dt][ft][2], kv[dt][ft][3]));
            }
        }
      }
          }
          const unsigned rowa = S2_OFF + ((u & 1) * 32 + r32) * S2_ROW + hh * 32 + hf * 16 + (u < 2 ? 288 : 144);
          const f32x16 z16 = {0.f, 0.f, 0.f, 0.f, 0.f, 0.f, 0.f, 0.f, 0.f, 0.f, 0.f, 0.f, 0.f, 0.f, 0.f, 0.f};
          const f32x16 pp = mma32(wp[1], lrd128(rowa + 32), mma32(wp[0], lrd128(rowa), z16));
          unsigned phv[10];
          hedgehog(pp, phv);
#pragma unroll
          for (int e = 0; e < 5; ++e) lwr64(PH_OFF + hh * 5120 + ((u & 1) * 32 + r32) * 80 + hf * 40 + e * 8, u32x2{phv[2 * e], phv[2 * e + 1]});
        }
    WAIT_L0();
    {
      LDS_AS unsigned* cnt = (LDS_AS unsigned*)(uintptr_t)(WPHI_OFF + 3068);
      if (lane == 0) __hip_atomic_fetch_add(cnt, 1u, __ATOMIC_RELAXED, __HIP_MEMORY_SCOPE_WORKGROUP);
      while (__builtin_amdgcn_readfirstlane(__hip_atomic_load(cnt, __ATOMIC_RELAXED, __HIP_MEMORY_SCOPE_WORKGROUP)) < 4u) __builtin_amdgcn_s_sleep(1);
    }
#pragma nounroll
        for (int s = 0; s < 2; ++s) issue_slab(slot0 + s * 4096, s1, FF * 2, rot + s, r8, chs);
      }
      STAMP(7);
      set_bias(acc, bia);
    } else if (ph == 1) {
      if (grpA) {
        if (w != 0) {
      const int col = i * 48 + (w - 1) * 16 + c16;
      unsigned pv[8];
      sweep<8>(ws + WS_KVP + ((size_t)(j * 32 + g * 8) * 1536 + (col < 1520 ? col : 0)) * 8, 1536 * 8, tag, pv);
      float a = 0.f, b = 0.f;
#pragma unroll
      for (int e = 0; e < 8; ++e) { a += bflo(pv[e]); b += bfhi(pv[e]); }
      a += __shfl_xor(a, 16); b += __shfl_xor(b, 16); a += __shfl_xor(a, 32); b += __shfl_xor(b, 32);
      if (g == 0) st_gran(ws + WS_KV + (size_t)(j * 1536 + col) * 8, tag, pk2(a, b));
      if (i == 0 && w == 1 && lane == 0) __hip_atomic_store((gu32*)(ws + WS_EP + j * 128), tag, __ATOMIC_RELAXED, __HIP_MEMORY_SCOPE_AGENT);
        }
        BAR();
      }
      write_act(act, xr0, xr1, w);
      if (w == 0) {
#pragma unroll
        for (int m = 0; m < 4; ++m) { lwr128(xr0 + 65536 + m * 1024, keep[m]); lwr128(xr1 + 65536 + m * 1024, keep[4 + m]); }
      }
      BAR();
      STAMP(9);
      set_zero(acc);
    } else if (ph == 2 && grpA) {
      BAR();
    } else {
      if (ph == 4) store_part(keep, Pb + lane * 16, w);
      pack_part(acc, keep);
      set_zero(acc);
    }
  }
  char* const P = Pb + lane * 16;
  if (grpA) {
#pragma unroll
    for (int e = 0; e < 8; ++e) lwr128(slot0 + e * 1024 + lane * 16, keep[e]);
    {
      const int r32 = lane & 31, hf = lane >> 5, reg = (r32 & 3) + 4 * (r32 >> 3);
      const int d = reg < 9 ? 9 * ((r32 >> 2) & 1) + reg : 18;
      unsigned kq[10];
      sweep<10>(ws + WS_KV + (size_t)(j * 1536 + w * 380 + d * 20 + hf * 10) * 8, 8, tag, kq);
      const int ch = 9 * j + 2 * w + hf, c8 = 9 * j + 8;
#pragma nounroll
      for (int mh = 0; mh < 2; ++mh) {
        const unsigned pa = PH_OFF + w * 5120 + (mh * 32 + r32) * 80 + hf * 40;
        const u32x2 q0 = lrd64(pa), q1 = lrd64(pa + 8), q2 = lrd64(pa + 16), q3 = lrd64(pa + 24), q4 = lrd64(pa + 32);
        f32x16 o = {0.f, 0.f, 0.f, 0.f, 0.f, 0.f, 0.f, 0.f, 0.f, 0.f, 0.f, 0.f, 0.f, 0.f, 0.f, 0.f};
        o = mma32(u32x4{kq[0], kq[1], kq[2], kq[3]}, u32x4{q0.x, q0.y, q1.x, q1.y}, o);
        o = mma32(u32x4{kq[4], kq[5], kq[6], kq[7]}, u32x4{q2.x, q2.y, q3.x, q3.y}, o);
        o = mma32(u32x4{kq[8], kq[9], 0u, 0u}, u32x4{q4.x, q4.y, 0u, 0u}, o);
        const float inv = 1.0f / (o[9] + 1e-6f);
        char* ob = ws + WS_O + ((size_t)(2 * i + mh) * 36 + (r32 >> 4)) * 1024 + (r32 & 15) * 16;
        *(u32x4*)(ob + (ch >> 2) * 2048 + (ch & 3) * 256) =
            u32x4{pk2(o[0] * inv, o[1] * inv), pk2(o[2] * inv, o[3] * inv), pk2(o[4] * inv, o[5] * inv), pk2(o[6] * inv, o[7] * inv)};
        *(u16*)(ob + (c8 >> 2) * 2048 + (c8 & 3) * 256 + (2 * w + hf) * 2) = (u16)pk2(o[8] * inv, 0.f);
      }
    }
  } else {
    store_part(keep, P, w);
  }
  STAMP(12);
  BAR();
  {
    float v[8] = {0.f, 0.f, 0.f, 0.f, 0.f, 0.f, 0.f, 0.f};
#pragma unroll
    for (int a = 0; a < 4; ++a) {
      const u32x4 u = lrd128(RING_OFF + a * 8192 + w * 1024 + lane * 16);
#pragma unroll
      for (int e = 0; e < 4; ++e) { v[2 * e] += bflo(u[e]); v[2 * e + 1] += bfhi(u[e]); }
    }
    *(u32x4*)(P + ((w & 1) * 36 + 32 + (w >> 1)) * 1024) = u32x4{pk2(v[0], v[1]), pk2(v[2], v[3]), pk2(v[4], v[5]), pk2(v[6], v[7])};
  }
  STAMP(13);
  STAMP(14); STAMP(15);
  STAMP_FLUSH(ws);
}

__global__ __launch_bounds__(576) void k2(const u16* __restrict__ x, const char* __restrict__ ws, u16* __restrict__ out) {
  __shared__ __attribute__((aligned(16))) char ob[36864];
  const int tid = threadIdx.x, lane = tid & 63, w = __builtin_amdgcn_readfirstlane(tid >> 6);
  const int b = blockIdx.x, i32 = (b & 7) + 8 * (b >> 5), nt = ((b >> 3) & 3) * 9 + w;
  const int c16 = lane & 15, g = lane >> 4;
  const unsigned obase = (unsigned)(uintptr_t)(LDS_AS char*)ob;
#pragma unroll
  for (int s = 0; s < 4; ++s) dma16(obase + (w * 4 + s) * 1024, lane * 16, ws + WS_O + ((size_t)i32 * 36 + w * 4 + s) * 1024);
  u32x4 wf[18], pp[8];
  u32x2 xr[2];
#pragma unroll
  for (int ks = 0; ks < 18; ++ks) wf[ks] = *(const u32x4*)(ws + WS_WOT + ((size_t)(ks * 36 + nt) * 64 + lane) * 16);
#pragma unroll
  for (int s = 0; s < 8; ++s) pp[s] = *(const u32x4*)(ws + WS_P + ((size_t)((s * 64 + i32) * 36 + nt) * 64 + lane) * 16);
#pragma unroll
  for (int m = 0; m < 2; ++m) xr[m] = *(const u32x2*)(x + (size_t)(i32 * 32 + m * 16 + c16) * 576 + nt * 16 + g * 4);
  WAIT_V(28);
  BAR();
  f32x4 acc[2] = {{0.f, 0.f, 0.f, 0.f}, {0.f, 0.f, 0.f, 0.f}};
#pragma unroll
  for (int ks = 0; ks < 18; ++ks)
#pragma unroll
    for (int m = 0; m < 2; ++m) acc[m] = mma16(wf[ks], lrd128(obase + (ks * 2 + m) * 1024 + lane * 16), acc[m]);
#pragma unroll
  for (int m = 0; m < 2; ++m) {
    float bs[4] = {0.f, 0.f, 0.f, 0.f};
#pragma unroll
    for (int s = 0; s < 8; ++s) {
      bs[0] += bflo(pp[s][2 * m]); bs[1] += bfhi(pp[s][2 * m]); bs[2] += bflo(pp[s][2 * m + 1]); bs[3] += bfhi(pp[s][2 * m + 1]);
    }
    const float xs[4] = {bflo(xr[m].x), bfhi(xr[m].x), bflo(xr[m].y), bfhi(xr[m].y)};
    float o[4];
#pragma unroll
    for (int r = 0; r < 4; ++r) {
      const float sg = __builtin_amdgcn_rcpf(1.0f + __builtin_amdgcn_exp2f(-1.4426950408889634f * bs[r]));
      o[r] = __builtin_fmaf(acc[m][r], bs[r] * sg, xs[r]);
    }
    *(u32x2*)(out + (size_t)(i32 * 32 + m * 16 + c16) * 576 + nt * 16 + g * 4) = u32x2{pk2(o[0], o[1]), pk2(o[2], o[3])};
  }
}

extern "C" void kernel_launch(void* const* d_in, const int* in_sizes, int n_in, void* d_out, int out_size, void* d_ws, size_t ws_size,
                              hipStream_t stream) {
  const u16* x = (const u16*)d_in[0];
  const u16* wqkv = (const u16*)d_in[1];
  const u16* wphi = (const u16*)d_in[2];
  const u16* wo = (const u16*)d_in[3];
  const u16* w1 = (const u16*)d_in[5];
  const u16* b1 = (const u16*)d_in[6];
  const u16* w2 = (const u16*)d_in[7];
  char* ws = (char*)d_ws;
  k1<<<256, 512, 0, stream>>>(x, wqkv, wphi, wo, w1, b1, w2, ws);
  k2<<<256, 576, 0, stream>>>(x, ws, (u16*)d_out);
}
```

```cpp
#include <hip/hip_runtime.h>
#include <hip/hip_bf16.h>
#include <stdint.h>

typedef __attribute__((ext_vector_type(8))) __bf16 bf16x8;
typedef __attribute__((ext_vector_type(2))) __bf16 bf16x2;
typedef __attribute__((ext_vector_type(4))) short s16x4;
typedef __attribute__((ext_vector_type(2))) float f32x2;
typedef __attribute__((ext_vector_type(4))) float f32x4;
typedef __attribute__((ext_vector_type(16))) float f32x16;
typedef __attribute__((ext_vector_type(4))) unsigned u32x4;
typedef __attribute__((ext_vector_type(2))) unsigned u32x2;
typedef unsigned short u16;
#define LDS_AS __attribute__((address_space(3)))
#define DEVI __device__ __forceinline__

constexpr int SEQ = 2048, DM = 576, FF = 4608;
constexpr int XA_OFF = 0;
constexpr int RING_OFF = 73728;
constexpr int S2_OFF = RING_OFF + 32768;
constexpr int S2_ROW = 464;
constexpr int WPHI_OFF = RING_OFF + 65536;
constexpr int PH_OFF = WPHI_OFF + 3072;
constexpr int DUMMY_OFF = PH_OFF + 20480;
constexpr int LDS_BYTES = 163840;
constexpr size_t WS_P = 0;
constexpr size_t WS_KVP = WS_P + (size_t)8 * 64 * 36 * 1024;
constexpr size_t WS_KV = WS_KVP + (size_t)8 * 32 * 1536 * 8;
constexpr size_t WS_EP = WS_KV + (size_t)8 * 1536 * 8;
constexpr size_t WS_O = WS_EP + 1024;
constexpr size_t WS_WOT = WS_O + (size_t)64 * 36 * 1024;

#define STAMP(n)
#define STAMP_DECL
#define STAMP_ARG
#define STAMP_PASS
#define STAMP_FLUSH(ws)
DEVI unsigned pk2(float a, float b) {
  f32x2 v = {a, b};
  return __builtin_bit_cast(unsigned, __builtin_convertvector(v, bf16x2));
}
DEVI float bflo(unsigned u) { return __builtin_bit_cast(float, u << 16); }
DEVI float bfhi(unsigned u) { return __builtin_bit_cast(float, u & 0xffff0000u); }
DEVI u32x2 trd(unsigned a) {
  return __builtin_bit_cast(u32x2, __builtin_amdgcn_ds_read_tr16_b64_v4i16((LDS_AS s16x4*)(uintptr_t)a));
}
DEVI u32x4 lrd128(unsigned a) { return *(LDS_AS u32x4*)(uintptr_t)a; }
DEVI u32x2 lrd64(unsigned a) { return *(LDS_AS u32x2*)(uintptr_t)a; }
DEVI void lwr128(unsigned a, u32x4 v) { *(LDS_AS u32x4*)(uintptr_t)a = v; }
DEVI void lwr64(unsigned a, u32x2 v) { *(LDS_AS u32x2*)(uintptr_t)a = v; }
DEVI f32x4 mma16(u32x4 a, u32x4 b, f32x4 c) {
  return __builtin_amdgcn_mfma_f32_16x16x32_bf16(__builtin_bit_cast(bf16x8, a), __builtin_bit_cast(bf16x8, b), c, 0, 0, 0);
}
DEVI f32x16 mma32(u32x4 a, u32x4 b, f32x16 c) {
  return __builtin_amdgcn_mfma_f32_32x32x16_bf16(__builtin_bit_cast(bf16x8, a), __builtin_bit_cast(bf16x8, b), c, 0, 0, 0);
}
DEVI void dma16(unsigned ldsb, unsigned voff, const char* base) {
  asm volatile("s_mov_b32 m0, %0\n\ts_nop 0\n\tglobal_load_lds_dwordx4 %1, %2" ::"s"(ldsb), "v"(voff), "s"(base) : "memory");
}
#define WAIT_V(n) asm volatile("s_waitcnt vmcnt(%0)" ::"i"(n) : "memory")
#define WAIT_L0() asm volatile("s_waitcnt lgkmcnt(0)" ::: "memory")
#define BAR() asm volatile("s_waitcnt lgkmcnt(0)\n\ts_barrier" ::: "memory")
typedef __attribute__((address_space(1))) unsigned long long gu64;
typedef __attribute__((address_space(1))) unsigned gu32;
DEVI void st_gran(char* p, unsigned tag, unsigned v) {
  __hip_atomic_store((gu64*)p, ((unsigned long long)tag << 32) | v, __ATOMIC_RELAXED, __HIP_MEMORY_SCOPE_AGENT);
}
template <int N>
DEVI void sweep(const char* p, int S, unsigned tag, unsigned (&v)[N]) {
  for (int spins = 0; spins < (1 << 16); ++spins) {
    bool ok = true;
#pragma unroll
    for (int k = 0; k < N; ++k) {
      const unsigned long long x = __hip_atomic_load((gu64*)(p + k * S), __ATOMIC_RELAXED, __HIP_MEMORY_SCOPE_AGENT);
      v[k] = (unsigned)x;
      ok &= (unsigned)(x >> 32) == tag;
    }
    if (__all(ok)) break;
    __builtin_amdgcn_s_sleep(8);
  }
}
#define SCHED() __builtin_amdgcn_sched_barrier(0)
DEVI f32x2 halves(float v) {
  const unsigned u = __builtin_bit_cast(unsigned, v);
  auto r = __builtin_amdgcn_permlane32_swap(u, u, false, false);
  const unsigned r0 = r[0], r1 = r[1];
  return f32x2{__builtin_bit_cast(float, r0), __builtin_bit_cast(float, r1)};
}

struct Strm { const char* base; int ld; unsigned voff; };
DEVI Strm mk_strm(const char* base, int ld, int lane) {
  return Strm{base, ld, (unsigned)((lane >> 3) * ld + ((lane & 7) ^ (((lane >> 4) & 3) << 1)) * 16)};
}
DEVI void issue_slab(unsigned slot, const Strm& s, int ks) {
  const char* rb = s.base + (size_t)(ks * 32 * s.ld);
#pragma unroll
  for (int i = 0; i < 4; ++i) dma16(slot + i * 1024, s.voff, rb + (size_t)(i * 8 * s.ld));
}
struct Frag { u32x4 w[4], x[4]; };
template <bool MK>
DEVI void read_frag(Frag& f, const unsigned (&ta)[4], unsigned soff, unsigned xaddr, int tm) {
#pragma unroll
  for (int t = 0; t < 4; ++t)
    if (!MK || (tm >> t & 1)) {
      const u32x2 lo = trd(ta[t] + soff), hi = trd(ta[t] + soff + 2048);
      f.w[t] = u32x4{lo.x, lo.y, hi.x, hi.y};
    }
#pragma unroll
  for (int t = 0; t < 4; ++t) f.x[t] = lrd128(xaddr + t * 1024);
}
template <bool MK, int T0, int T1>
DEVI void mma_part(f32x4 (&acc)[4][4], const Frag& f, int tm) {
#pragma unroll
  for (int t = T0; t < T1; ++t)
    if (!MK || (tm >> t & 1)) {
#pragma unroll
      for (int m = 0; m < 4; ++m) acc[t][m] = mma16(f.w[t], f.x[m], acc[t][m]);
    }
}
DEVI int wrap18(int k) { return k >= 18 ? k - 18 : k; }
template <bool MK>
DEVI void gemm(f32x4 (&acc)[4][4], const unsigned (&ta)[4], unsigned xr0, unsigned xr1, unsigned slot0, const Strm& cur, int k0, int n,
               bool has_next, const Strm& nx, int nk0, int tm = 15) {
  Frag f0, f1;
  if (MK) {
#pragma unroll
    for (int t = 0; t < 4; ++t) f0.w[t] = f1.w[t] = u32x4{0u, 0u, 0u, 0u};
  }
  WAIT_V(4);
  read_frag<MK>(f0, ta, 0, xr0 + k0 * 4096, tm);
  WAIT_L0();
  int k = k0;
  issue_slab(slot0, cur, wrap18(k + 2));
  for (int it = 2; it < n; it += 2) {
    const int k2 = wrap18(k + 2);
    WAIT_V(4);
    read_frag<MK>(f1, ta, 4096, xr1 + k * 4096, tm);
    SCHED();
    mma_part<MK, 0, 2>(acc, f0, tm);
    SCHED();
    WAIT_L0();
    issue_slab(slot0 + 4096, cur, k2 + 1);
    SCHED();
    mma_part<MK, 2, 4>(acc, f0, tm);
    SCHED();
    WAIT_V(4);
    read_frag<MK>(f0, ta, 0, xr0 + k2 * 4096, tm);
    SCHED();
    mma_part<MK, 0, 2>(acc, f1, tm);
    SCHED();
    WAIT_L0();
    if (it + 2 < n) issue_slab(slot0, cur, wrap18(k2 + 2));
    else if (has_next) issue_slab(slot0, nx, nk0);
    SCHED();
    mma_part<MK, 2, 4>(acc, f1, tm);
    SCHED();
    k = k2;
  }
  if (has_next) WAIT_V(4); else WAIT_V(0);
  read_frag<MK>(f1, ta, 4096, xr1 + k * 4096, tm);
  SCHED();
  mma_part<MK, 0, 2>(acc, f0, tm);
  SCHED();
  WAIT_L0();
  if (has_next) issue_slab(slot0 + 4096, nx, nk0 + 1);
  SCHED();
  mma_part<MK, 2, 4>(acc, f0, tm);
  mma_part<MK, 0, 4>(acc, f1, tm);
}

DEVI float mish(float h) {
  float e = __builtin_amdgcn_exp2f(h * 1.4426950408889634f);
  float d = __builtin_fmaf(e, e + 2.0f, 2.0f);
  float r = __builtin_amdgcn_rcpf(d);
  return h * __builtin_fmaf(-2.0f, r, 1.0f);
}

DEVI void set_bias(f32x4 (&acc)[4][4], const u32x2 (&b)[4]) {
#pragma unroll
  for (int t = 0; t < 4; ++t)
#pragma unroll
    for (int m = 0; m < 4; ++m) acc[t][m] = f32x4{bflo(b[t].x), bfhi(b[t].x), bflo(b[t].y), bfhi(b[t].y)};
}
DEVI void set_zero(f32x4 (&acc)[4][4]) {
#pragma unroll
  for (int t = 0; t < 4; ++t)
#pragma unroll
    for (int m = 0; m < 4; ++m) acc[t][m] = f32x4{0.f, 0.f, 0.f, 0.f};
}
DEVI void mish_pack(const f32x4 (&acc)[4][4], u32x4 (&act)[2][4]) {
#pragma unroll
  for (int h = 0; h < 2; ++h)
#pragma unroll
    for (int m = 0; m < 4; ++m) {
      act[h][m] = u32x4{pk2(mish(acc[2 * h][m][0]), mish(acc[2 * h][m][1])), pk2(mish(acc[2 * h][m][2]), mish(acc[2 * h][m][3])),
                        pk2(mish(acc[2 * h + 1][m][0]), mish(acc[2 * h + 1][m][1])), pk2(mish(acc[2 * h + 1][m][2]), mish(acc[2 * h + 1][m][3]))};
      asm volatile("" : "+v"(act[h][m]));
    }
}
DEVI void write_act(const u32x4 (&act)[2][4], unsigned xr0, unsigned xr1, int blk) {
#pragma unroll
  for (int m = 0; m < 4; ++m) { lwr128(xr0 + blk * 8192 + m * 1024, act[0][m]); lwr128(xr1 + blk * 8192 + m * 1024, act[1][m]); }
}
DEVI void pack_part(const f32x4 (&acc)[4][4], u32x4 (&o)[8]) {
#pragma unroll
  for (int t = 0; t < 4; ++t)
#pragma unroll
    for (int h = 0; h < 2; ++h) {
      o[t * 2 + h] = u32x4{pk2(acc[t][2 * h][0], acc[t][2 * h][1]), pk2(acc[t][2 * h][2], acc[t][2 * h][3]),
                           pk2(acc[t][2 * h + 1][0], acc[t][2 * h + 1][1]), pk2(acc[t][2 * h + 1][2], acc[t][2 * h + 1][3])};
      asm volatile("" : "+v"(o[t * 2 + h]));
    }
}
DEVI void store_part(const u32x4 (&o)[8], char* P, int blk) {
#pragma unroll
  for (int e = 0; e < 8; ++e) __builtin_nontemporal_store(o[e], (u32x4*)(P + ((e & 1) * 36 + blk * 4 + (e >> 1)) * 1024));
}

DEVI void hedgehog(const f32x16& p, unsigned (&out)[10]) {
  float mx = p[0], mn = p[0];
#pragma unroll
  for (int r = 1; r < 9; ++r) { mx = fmaxf(mx, p[r]); mn = fminf(mn, p[r]); }
  { const f32x2 a = halves(mx), b = halves(mn); mx = fmaxf(a.x, a.y); mn = fminf(b.x, b.y); }
  const float L2E = 1.4426950408889634f;
  const float mxl = mx * L2E, mnl = mn * L2E;
  float e[18], sp = 0.f, sn = 0.f;
#pragma unroll
  for (int r = 0; r < 9; ++r) {
    e[r] = __builtin_amdgcn_exp2f(__builtin_fmaf(p[r], L2E, -mxl));
    e[9 + r] = __builtin_amdgcn_exp2f(__builtin_fmaf(p[r], -L2E, mnl));
    sp += e[r]; sn += e[9 + r];
  }
  { const f32x2 a = halves(sp), b = halves(sn); sp = a.x + a.y; sn = b.x + b.y; }
  const float ip = __builtin_amdgcn_rcpf(sp), in = __builtin_amdgcn_rcpf(sn);
#pragma unroll
  for (int r = 0; r < 9; ++r) { e[r] *= ip; e[9 + r] *= in; }
#pragma unroll
  for (int r = 0; r < 9; ++r) out[r] = pk2(e[2 * r], e[2 * r + 1]);
  out[9] = 0u;
}

__global__ __launch_bounds__(512, 2) void k1(const u16* __restrict__ x, const u16* __restrict__ wqkv, const u16* __restrict__ wphi,
                                             const u16* __restrict__ wo, const u16* __restrict__ w1, const u16* __restrict__ b1,
                                             const u16* __restrict__ w2, char* __restrict__ ws) {
  __shared__ __attribute__((aligned(16))) char smem[LDS_BYTES];
  asm volatile("" ::"v"((unsigned)(uintptr_t)(LDS_AS char*)smem));
  const int tid = threadIdx.x, lane = tid & 63, w = __builtin_amdgcn_readfirstlane(tid >> 6);
  const int j = (blockIdx.x & 6) | ((blockIdx.x >> 3) & 1), i = (blockIdx.x >> 4) | ((blockIdx.x & 1) << 4);
  const int c16 = lane & 15, g = lane >> 4;
  const unsigned slot0 = RING_OFF + w * 8192;
  STAMP_DECL;
  STAMP(0);
  const int r8 = lane >> 3, sub = lane & 7;
  const int mrow = w * 8 + ((r8 >> 1) | ((r8 & 1) << 2));
  u32x4 xv[9];
  {
    const unsigned vo = mrow * 1152 + sub * 16;
    const char* xb = (const char*)x + (size_t)i * 64 * 1152;
#pragma unroll
    for (int c = 0; c < 9; ++c) asm volatile("global_load_dwordx4 %0, %1, %2 offset:%3" : "=v"(xv[c]) : "v"(vo), "s"(xb), "i"(c * 128) : "memory");
  }
  u32x2 bia[4], bi8[4];
  {
    const char* bb = (const char*)b1 + j * 1152;
    const unsigned vo = w * 128 + g * 8, v8 = 1024 + g * 8;
#pragma unroll
    for (int t = 0; t < 4; ++t) {
      asm volatile("global_load_dwordx2 %0, %1, %2 offset:%3" : "=v"(bia[t]) : "v"(vo), "s"(bb), "i"(t * 32) : "memory");
      asm volatile("global_load_dwordx2 %0, %1, %2 offset:%3" : "=v"(bi8[t]) : "v"(v8), "s"(bb), "i"(t * 32) : "memory");
    }
  }
  unsigned tagv;
  asm volatile("global_load_dword %0, %1, %2 sc1" : "=v"(tagv) : "v"(j * 128), "s"(ws + WS_EP) : "memory");
  asm volatile("s_barrier" ::: "memory");
  const int rot = 2 * (i % 9);
  const int part = (w - 1) >> 1, line = (w - 1) & 1;
  const Strm s1 = mk_strm((const char*)w1 + j * 1152 + w * 128, FF * 2, lane);
  const Strm sr = w == 0 ? mk_strm((const char*)w1 + j * 1152 + 1024, FF * 2, lane)
                         : mk_strm((const char*)wqkv + (part == 0 ? 2304 : (part - 1) * 1152) + (j + line) * 128, 3456, lane);
  if (w < 7) {
    issue_slab(slot0, sr, rot);
    issue_slab(slot0 + 4096, sr, rot + 1);
  } else {
#pragma unroll
    for (int s = 0; s < 3; ++s) { int c = s * 64 + lane; c = c > 161 ? 161 : c; dma16(WPHI_OFF + s * 1024, c * 16, (const char*)wphi + j * 2592); }
#pragma unroll
    for (int s = 0; s < 5; ++s) dma16(DUMMY_OFF, (j * 576 + (i & 15) * 36) * 1152 + (s * 64 + lane) * 128, (const char*)w2);
  }
  STAMP(1);
  asm volatile("s_waitcnt vmcnt(8)" : "+v"(xv[0]), "+v"(xv[1]), "+v"(xv[2]), "+v"(xv[3]), "+v"(xv[4]), "+v"(xv[5]), "+v"(xv[6]), "+v"(xv[7]), "+v"(xv[8]),
               "+v"(bia[0]), "+v"(bia[1]), "+v"(bia[2]), "+v"(bia[3]), "+v"(bi8[0]), "+v"(bi8[1]), "+v"(bi8[2]), "+v"(bi8[3]), "+v"(tagv)::"memory");
  const unsigned tag = __builtin_amdgcn_readfirstlane(tagv) + 1u;
  {
    STAMP(2);
    float s = 0.f, v = 0.f;
    const bf16x2 one2 = __builtin_bit_cast(bf16x2, 0x3f803f80u);
#pragma unroll
    for (int c = 0; c < 9; ++c)
#pragma unroll
      for (int e = 0; e < 4; ++e) {
        const unsigned xu = xv[c][e];
        const bf16x2 xx = __builtin_bit_cast(bf16x2, xu);
        s = __builtin_amdgcn_fdot2_f32_bf16(xx, one2, s, false);
        v = __builtin_amdgcn_fdot2_f32_bf16(xx, xx, v, false);
      }
    s += __shfl_xor(s, 1); v += __shfl_xor(v, 1); s += __shfl_xor(s, 2); v += __shfl_xor(v, 2); s += __shfl_xor(s, 4); v += __shfl_xor(v, 4);
    const float mean = s * (1.0f / 576.0f);
    const float var = fmaxf(__builtin_fmaf(-mean, mean, v * (1.0f / 576.0f)), 0.f);
    const float rs = __builtin_amdgcn_rsqf(var + 1e-5f), nb = -mean * rs;
    const int f = ((sub >> 2) << 1) + (sub & 1);
    const unsigned wa = XA_OFF + (sub >> 2) * 4096 + (mrow >> 4) * 1024 + ((sub & 1) * 32 + ((mrow & 15) ^ f)) * 16 + ((sub >> 1) & 1) * 8;
#pragma unroll
    for (int c = 0; c < 9; ++c) {
      unsigned o[4];
#pragma unroll
      for (int e = 0; e < 4; ++e) o[e] = pk2(__builtin_fmaf(bflo(xv[c][e]), rs, nb), __builtin_fmaf(bfhi(xv[c][e]), rs, nb));
      lwr64(wa + c * 8192, u32x2{o[0], o[1]});
      lwr64(wa + c * 8192 + 256, u32x2{o[2], o[3]});
    }
  }
  STAMP(3);
  BAR();
  STAMP(4);
  const unsigned xr0 = XA_OFF + (g * 16 + (c16 ^ (g >> 1))) * 16, xr1 = XA_OFF + 4096 + (g * 16 + (c16 ^ (2 + (g >> 1)))) * 16;
  unsigned ta[4];
  {
    const int q = (lane >> 2) & 3, p = lane & 3, r = 4 * g + q, s = (r >> 1) & 3;
#pragma unroll
    for (int t = 0; t < 4; ++t) ta[t] = slot0 + 128 * r + 32 * (t ^ s) + 8 * p;
  }
  f32x4 acc[4][4];
  if (w == 0) set_bias(acc, bi8); else set_zero(acc);
  if (w < 7) {
    const int tm = w == 0 ? 15 : (line == 0 ? (15 << (j >> 1)) & 15 : 15 >> (3 - (j >> 1)));
    gemm<true>(acc, ta, xr0, xr1, slot0, sr, rot, 18, w < 4, s1, rot, tm);
  } else {
    dma16(DUMMY_OFF, (j * 576 + (i & 15) * 36) * 1152 + (320 + (lane & 3)) * 128, (const char*)w2);
#pragma unroll
    for (int s = 0; s < 3; ++s) {
      const int fr = blockIdx.x + s * 256;
      if (fr < 648) {
        const int ks = fr / 36, nt = fr - ks * 36;
        const int ch = ks * 4 + g, c9 = ch % 9;
        const u16* src = wo + ((ch / 9) * 72 + (c9 < 8 ? 9 * c9 : 8)) * 576 + nt * 16 + c16;
        const int st = c9 < 8 ? 576 : 9 * 576;
        u32x4 o;
#pragma unroll
        for (int e = 0; e < 4; ++e) o[e] = (unsigned)src[(2 * e) * st] | ((unsigned)src[(2 * e + 1) * st] << 16);
        *(u32x4*)(ws + WS_WOT + ((size_t)fr * 64 + lane) * 16) = o;
      }
    }
    WAIT_V(0);
  }
  STAMP(5);
  BAR();
  if (w == 7) {
    lwr128(S2_OFF + lane * S2_ROW + 432, u32x4{0u, 0u, 0u, 0u});
    *(LDS_AS unsigned*)(uintptr_t)(WPHI_OFF + 3068) = 0u;
  } else if (w > 0) {
#pragma unroll
    for (int t = 0; t < 4; ++t) {
      const int lc = 64 * line + 16 * t + 4 * g - 8 * j;
      if (lc >= 0 && lc < 72) {
#pragma unroll
        for (int m = 0; m < 4; ++m)
          lwr64(S2_OFF + (m * 16 + c16) * S2_ROW + part * 144 + lc * 2, u32x2{pk2(acc[t][m][0], acc[t][m][1]), pk2(acc[t][m][2], acc[t][m][3])});
      }
    }
  }
  BAR();
  STAMP(6);
  const Strm s2 = mk_strm((const char*)w2 + (size_t)j * 576 * 1152 + w * 128, DM * 2, lane);
  char* P = ws + WS_P + ((size_t)(j * 64 + 2 * i) * 36 * 64 + lane) * 16;
  u32x4 act[2][4];
  if (w < 4) {
    u32x4 act8[2][4];
    if (w == 0) mish_pack(acc, act8);
    set_bias(acc, bia);
    gemm<false>(acc, ta, xr0, xr1, slot0, s1, rot, 18, true, s2, 16);
    STAMP(7);
    mish_pack(acc, act);
    if (w != 0) {
      const int col = i * 48 + (w - 1) * 16 + c16;
      unsigned pv[8];
      sweep<8>(ws + WS_KVP + ((size_t)(j * 32 + g * 8) * 1536 + (col < 1520 ? col : 0)) * 8, 1536 * 8, tag, pv);
      float a = 0.f, b = 0.f;
#pragma unroll
      for (int e = 0; e < 8; ++e) { a += bflo(pv[e]); b += bfhi(pv[e]); }
      a += __shfl_xor(a, 16); b += __shfl_xor(b, 16); a += __shfl_xor(a, 32); b += __shfl_xor(b, 32);
      if (g == 0) st_gran(ws + WS_KV + (size_t)(j * 1536 + col) * 8, tag, pk2(a, b));
      if (i == 0 && w == 1 && lane == 0) __hip_atomic_store((gu32*)(ws + WS_EP + j * 128), tag, __ATOMIC_RELAXED, __HIP_MEMORY_SCOPE_AGENT);
    }
    STAMP(8);
    BAR();
    write_act(act, xr0, xr1, w);
    if (w == 0) write_act(act8, xr0, xr1, 8);
    BAR();
    STAMP(9);
    set_zero(acc);
    gemm<false>(acc, ta, xr0, xr1, slot0, s2, 16, 10, true, s2, 8);
    STAMP(10);
    BAR();
    const Strm s8 = mk_strm((const char*)w2 + (size_t)j * 576 * 1152 + 1024, DM * 2, lane);
    gemm<false>(acc, ta, xr0, xr1, slot0, s2, 8, 8, true, s8, 4 * w);
    STAMP(11);
    u32x4 po[8], p8[8];
    pack_part(acc, po);
    set_zero(acc);
    gemm<false>(acc, ta, xr0, xr1, slot0, s8, 4 * w, w == 3 ? 6 : 4, false, s8, 0);
    store_part(po, P, w);
    pack_part(acc, p8);
#pragma unroll
    for (int e = 0; e < 8; ++e) lwr128(slot0 + e * 1024 + lane * 16, p8[e]);
    {
      const int r32 = lane & 31, hf = lane >> 5, reg = (r32 & 3) + 4 * (r32 >> 3);
      const int d = reg < 9 ? 9 * ((r32 >> 2) & 1) + reg : 18;
      unsigned kq[10];
      sweep<10>(ws + WS_KV + (size_t)(j * 1536 + w * 380 + d * 20 + hf * 10) * 8, 8, tag, kq);
      const int ch = 9 * j + 2 * w + hf, c8 = 9 * j + 8;
#pragma unroll
      for (int mh = 0; mh < 2; ++mh) {
        const unsigned pa = PH_OFF + w * 5120 + (mh * 32 + r32) * 80 + hf * 40;
        const u32x2 q0 = lrd64(pa), q1 = lrd64(pa + 8), q2 = lrd64(pa + 16), q3 = lrd64(pa + 24), q4 = lrd64(pa + 32);
        f32x16 o = {0.f, 0.f, 0.f, 0.f, 0.f, 0.f, 0.f, 0.f, 0.f, 0.f, 0.f, 0.f, 0.f, 0.f, 0.f, 0.f};
        o = mma32(u32x4{kq[0], kq[1], kq[2], kq[3]}, u32x4{q0.x, q0.y, q1.x, q1.y}, o);
        o = mma32(u32x4{kq[4], kq[5], kq[6], kq[7]}, u32x4{q2.x, q2.y, q3.x, q3.y}, o);
        o = mma32(u32x4{kq[8], kq[9], 0u, 0u}, u32x4{q4.x, q4.y, 0u, 0u}, o);
        const float inv = 1.0f / (o[9] + 1e-6f);
        char* ob = ws + WS_O + ((size_t)(2 * i + mh) * 36 + (r32 >> 4)) * 1024 + (r32 & 15) * 16;
        *(u32x4*)(ob + (ch >> 2) * 2048 + (ch & 3) * 256) =
            u32x4{pk2(o[0] * inv, o[1] * inv), pk2(o[2] * inv, o[3] * inv), pk2(o[4] * inv, o[5] * inv), pk2(o[6] * inv, o[7] * inv)};
        *(u16*)(ob + (c8 >> 2) * 2048 + (c8 & 3) * 256 + (2 * w + hf) * 2) = (u16)pk2(o[8] * inv, 0.f);
      }
    }
  } else {
    const int hh = w & 3, r32 = lane & 31, hf = lane >> 5;
    u32x4 wp[2];
    {
      const int reg = (r32 & 3) + 4 * (r32 >> 3), f = 9 * ((r32 >> 2) & 1) + reg;
#pragma unroll
      for (int s = 0; s < 2; ++s)
#pragma unroll
        for (int e = 0; e < 4; ++e) {
          unsigned v2[2];
#pragma unroll
          for (int b = 0; b < 2; ++b) {
            const int d = 16 * s + 8 * hf + 2 * e + b - 2 * hh;
            const bool ok = reg < 9 && d >= 0 && d < 18;
            const unsigned val = *(LDS_AS u16*)(uintptr_t)(WPHI_OFF + (ok ? (hh * 324 + d * 18 + f) * 2 : 0));
            v2[b] = ok ? val : 0u;
          }
          wp[s][e] = v2[0] | (v2[1] << 16);
        }
    }
    unsigned pq[2][10];
    const f32x16 z16 = {0.f, 0.f, 0.f, 0.f, 0.f, 0.f, 0.f, 0.f, 0.f, 0.f, 0.f, 0.f, 0.f, 0.f, 0.f, 0.f};
#pragma unroll
    for (int mh = 0; mh < 2; ++mh) {
      const unsigned rowa = S2_OFF + (mh * 32 + r32) * S2_ROW + hh * 32 + hf * 16;
      const f32x16 pk = mma32(wp[1], lrd128(rowa + 288 + 32), mma32(wp[0], lrd128(rowa + 288), z16));
      const f32x16 pqa = mma32(wp[1], lrd128(rowa + 144 + 32), mma32(wp[0], lrd128(rowa + 144), z16));
      unsigned ph[10];
      hedgehog(pk, ph);
#pragma unroll
      for (int e = 0; e < 5; ++e) lwr64(PH_OFF + hh * 5120 + (mh * 32 + r32) * 80 + hf * 40 + e * 8, u32x2{ph[2 * e], ph[2 * e + 1]});
      hedgehog(pqa, pq[mh]);
    }
    {
      const int q = (lane >> 2) & 3, p = lane & 3;
      const unsigned va = S2_OFF + (8 * g + q) * S2_ROW + 8 * p + hh * 32, pa = PH_OFF + hh * 5120 + (8 * g + q) * 80 + 8 * p;
      f32x4 kv[2][3];
#pragma unroll
      for (int e = 0; e < 6; ++e) kv[e / 3][e % 3] = f32x4{0.f, 0.f, 0.f, 0.f};
#pragma unroll
      for (int s = 0; s < 2; ++s) {
        u32x4 vf[2];
#pragma unroll
        for (int dt = 0; dt < 2; ++dt) {
          const u32x2 v0 = trd(va + s * 32 * S2_ROW + dt * 32), v1 = trd(va + (s * 32 + 4) * S2_ROW + dt * 32);
          vf[dt] = u32x4{v0.x, v0.y, v1.x, v1.y};
        }
        if (c16 == 15) vf[1] = u32x4{0x3f803f80u, 0x3f803f80u, 0x3f803f80u, 0x3f803f80u};
#pragma unroll
        for (int ft = 0; ft < 3; ++ft) {
          const u32x2 p0 = trd(pa + s * 32 * 80 + ft * 32), p1 = trd(pa + (s * 32 + 4) * 80 + ft * 32);
          const u32x4 pf = {p0.x, p0.y, p1.x, p1.y};
          kv[0][ft] = mma16(pf, vf[0], kv[0][ft]);
          kv[1][ft] = mma16(pf, vf[1], kv[1][ft]);
        }
      }
#pragma unroll
      for (int dt = 0; dt < 2; ++dt) {
        const int col = dt * 16 + c16, d = col == 31 ? 18 : col - 2 * hh;
        if (d >= 0 && (d < 18 || col == 31)) {
          char* dst = ws + WS_KVP + ((size_t)(j * 32 + i) * 1536 + hh * 380 + d * 20 + 2 * g) * 8;
#pragma unroll
          for (int ft = 0; ft < 3; ++ft)
            if (ft < 2 || g < 2) {
              st_gran(dst + ft * 64, tag, pk2(kv[dt][ft][0], kv[dt][ft][1]));
              st_gran(dst + ft * 64 + 8, tag, pk2(kv[dt][ft][2], kv[dt][ft][3]));
            }
        }
      }
#pragma unroll
      for (int mh = 0; mh < 2; ++mh)
#pragma unroll
        for (int e = 0; e < 5; ++e) lwr64(PH_OFF + hh * 5120 + (mh * 32 + r32) * 80 + hf * 40 + e * 8, u32x2{pq[mh][2 * e], pq[mh][2 * e + 1]});
    }
    WAIT_L0();
    {
      LDS_AS unsigned* cnt = (LDS_AS unsigned*)(uintptr_t)(WPHI_OFF + 3068);
      if (lane == 0) __hip_atomic_fetch_add(cnt, 1u, __ATOMIC_RELAXED, __HIP_MEMORY_SCOPE_WORKGROUP);
      while (__builtin_amdgcn_readfirstlane(__hip_atomic_load(cnt, __ATOMIC_RELAXED, __HIP_MEMORY_SCOPE_WORKGROUP)) < 4u) __builtin_amdgcn_s_sleep(1);
    }
    STAMP(7);
    issue_slab(slot0, s1, rot);
    issue_slab(slot0 + 4096, s1, rot + 1);
    set_bias(acc, bia);
    gemm<false>(acc, ta, xr0, xr1, slot0, s1, rot, 18, true, s2, 0);
    STAMP(8);
    BAR();
    BAR();
    STAMP(9);
    mish_pack(acc, act);
    write_act(act, xr0, xr1, w);
    STAMP(10);
    BAR();
    set_zero(acc);
    gemm<false>(acc, ta, xr0, xr1, slot0, s2, 0, 18, false, s2, 0);
    STAMP(11);
    u32x4 po[8];
    pack_part(acc, po);
    store_part(po, P, w);
  }
  STAMP(12);
  BAR();
  {
    float v[8] = {0.f, 0.f, 0.f, 0.f, 0.f, 0.f, 0.f, 0.f};
#pragma unroll
    for (int a = 0; a < 4; ++a) {
      const u32x4 u = lrd128(RING_OFF + a * 8192 + w * 1024 + lane * 16);
#pragma unroll
      for (int e = 0; e < 4; ++e) { v[2 * e] += bflo(u[e]); v[2 * e + 1] += bfhi(u[e]); }
    }
    *(u32x4*)(P + ((w & 1) * 36 + 32 + (w >> 1)) * 1024) = u32x4{pk2(v[0], v[1]), pk2(v[2], v[3]), pk2(v[4], v[5]), pk2(v[6], v[7])};
  }
  STAMP(13);
  STAMP(14); STAMP(15);
  STAMP_FLUSH(ws);
}

__global__ __launch_bounds__(576) void k2(const u16* __restrict__ x, const char* __restrict__ ws, u16* __restrict__ out) {
  __shared__ __attribute__((aligned(16))) char ob[36864];
  const int tid = threadIdx.x, lane = tid & 63, w = __builtin_amdgcn_readfirstlane(tid >> 6);
  const int b = blockIdx.x, i32 = (b & 7) + 8 * (b >> 5), nt = ((b >> 3) & 3) * 9 + w;
  const int c16 = lane & 15, g = lane >> 4;
  const unsigned obase = (unsigned)(uintptr_t)(LDS_AS char*)ob;
#pragma unroll
  for (int s = 0; s < 4; ++s) dma16(obase + (w * 4 + s) * 1024, lane * 16, ws + WS_O + ((size_t)i32 * 36 + w * 4 + s) * 1024);
  u32x4 wf[18], pp[8];
  u32x2 xr[2];
#pragma unroll
  for (int s = 0; s < 8; ++s) pp[s] = *(const u32x4*)(ws + WS_P + ((size_t)((s * 64 + i32) * 36 + nt) * 64 + lane) * 16);
  asm volatile("" ::: "memory");
#pragma unroll
  for (int ks = 0; ks < 18; ++ks) wf[ks] = *(const u32x4*)(ws + WS_WOT + ((size_t)(ks * 36 + nt) * 64 + lane) * 16);
#pragma unroll
  for (int m = 0; m < 2; ++m) xr[m] = *(const u32x2*)(x + (size_t)(i32 * 32 + m * 16 + c16) * 576 + nt * 16 + g * 4);
  WAIT_V(28);
  BAR();
  f32x4 acc[2] = {{0.f, 0.f, 0.f, 0.f}, {0.f, 0.f, 0.f, 0.f}};
#pragma unroll
  for (int ks = 0; ks < 18; ++ks)
#pragma unroll
    for (int m = 0; m < 2; ++m) acc[m] = mma16(wf[ks], lrd128(obase + (ks * 2 + m) * 1024 + lane * 16), acc[m]);
#pragma unroll
  for (int m = 0; m < 2; ++m) {
    float bs[4] = {0.f, 0.f, 0.f, 0.f};
#pragma unroll
    for (int s = 0; s < 8; ++s) {
      bs[0] += bflo(pp[s][2 * m]); bs[1] += bfhi(pp[s][2 * m]); bs[2] += bflo(pp[s][2 * m + 1]); bs[3] += bfhi(pp[s][2 * m + 1]);
    }
    const float xs[4] = {bflo(xr[m].x), bfhi(xr[m].x), bflo(xr[m].y), bfhi(xr[m].y)};
    float o[4];
#pragma unroll
    for (int r = 0; r < 4; ++r) {
      const float sg = __builtin_amdgcn_rcpf(1.0f + __builtin_amdgcn_exp2f(-1.4426950408889634f * bs[r]));
      o[r] = __builtin_fmaf(acc[m][r], bs[r] * sg, xs[r]);
    }
    *(u32x2*)(out + (size_t)(i32 * 32 + m * 16 + c16) * 576 + nt * 16 + g * 4) = u32x2{pk2(o[0], o[1]), pk2(o[2], o[3])};
  }
}

extern "C" void kernel_launch(void* const* d_in, const int* in_sizes, int n_in, void* d_out, int out_size, void* d_ws, size_t ws_size,
                              hipStream_t stream) {
  const u16* x = (const u16*)d_in[0];
  const u16* wqkv = (const u16*)d_in[1];
  const u16* wphi = (const u16*)d_in[2];
  const u16* wo = (const u16*)d_in[3];
  const u16* w1 = (const u16*)d_in[5];
  const u16* b1 = (const u16*)d_in[6];
  const u16* w2 = (const u16*)d_in[7];
  char* ws = (char*)d_ws;
  k1<<<256, 512, 0, stream>>>(x, wqkv, wphi, wo, w1, b1, w2, ws);
  k2<<<256, 576, 0, stream>>>(x, ws, (u16*)d_out);
}
```

```cpp
#include <hip/hip_runtime.h>
#include <hip/hip_bf16.h>
#include <stdint.h>

typedef __attribute__((ext_vector_type(8))) __bf16 bf16x8;
typedef __attribute__((ext_vector_type(2))) __bf16 bf16x2;
typedef __attribute__((ext_vector_type(4))) short s16x4;
typedef __attribute__((ext_vector_type(2))) float f32x2;
typedef __attribute__((ext_vector_type(4))) float f32x4;
typedef __attribute__((ext_vector_type(16))) float f32x16;
typedef __attribute__((ext_vector_type(4))) unsigned u32x4;
typedef __attribute__((ext_vector_type(2))) unsigned u32x2;
typedef unsigned short u16;
#define LDS_AS __attribute__((address_space(3)))
#define DEVI __device__ __forceinline__

constexpr int SEQ = 2048, DM = 576, FF = 4608;
constexpr int XA_OFF = 0;
constexpr int RING_OFF = 73728;
constexpr int S2_OFF = RING_OFF + 32768;
constexpr int S2_ROW = 464;
constexpr int WPHI_OFF = RING_OFF + 65536;
constexpr int PH_OFF = WPHI_OFF + 3072;
constexpr int DUMMY_OFF = PH_OFF + 20480;
constexpr int LDS_BYTES = 163840;
constexpr size_t WS_P = 0;
constexpr size_t WS_KVP = WS_P + (size_t)8 * 64 * 36 * 1024;
constexpr size_t WS_KV = WS_KVP + (size_t)8 * 32 * 1536 * 8;
constexpr size_t WS_EP = WS_KV + (size_t)8 * 1536 * 8;
constexpr size_t WS_O = WS_EP + 1024;
constexpr size_t WS_WOT = WS_O + (size_t)64 * 36 * 1024;

#define STAMP(n)
#define STAMP_DECL
#define STAMP_ARG
#define STAMP_PASS
#define STAMP_FLUSH(ws)
DEVI unsigned pk2(float a, float b) {
  f32x2 v = {a, b};
  return __builtin_bit_cast(unsigned, __builtin_convertvector(v, bf16x2));
}
DEVI float bflo(unsigned u) { return __builtin_bit_cast(float, u << 16); }
DEVI float bfhi(unsigned u) { return __builtin_bit_cast(float, u & 0xffff0000u); }
DEVI u32x2 trd(unsigned a) {
  return __builtin_bit_cast(u32x2, __builtin_amdgcn_ds_read_tr16_b64_v4i16((LDS_AS s16x4*)(uintptr_t)a));
}
DEVI u32x4 lrd128(unsigned a) { return *(LDS_AS u32x4*)(uintptr_t)a; }
DEVI u32x2 lrd64(unsigned a) { return *(LDS_AS u32x2*)(uintptr_t)a; }
DEVI void lwr128(unsigned a, u32x4 v) { *(LDS_AS u32x4*)(uintptr_t)a = v; }
DEVI void lwr64(unsigned a, u32x2 v) { *(LDS_AS u32x2*)(uintptr_t)a = v; }
DEVI f32x4 mma16(u32x4 a, u32x4 b, f32x4 c) {
  return __builtin_amdgcn_mfma_f32_16x16x32_bf16(__builtin_bit_cast(bf16x8, a), __builtin_bit_cast(bf16x8, b), c, 0, 0, 0);
}
DEVI f32x16 mma32(u32x4 a, u32x4 b, f32x16 c) {
  return __builtin_amdgcn_mfma_f32_32x32x16_bf16(__builtin_bit_cast(bf16x8, a), __builtin_bit_cast(bf16x8, b), c, 0, 0, 0);
}
DEVI void dma16(unsigned ldsb, unsigned voff, const char* base) {
  asm volatile("s_mov_b32 m0, %0\n\ts_nop 0\n\tglobal_load_lds_dwordx4 %1, %2" ::"s"(ldsb), "v"(voff), "s"(base) : "memory");
}
#define WAIT_V(n) asm volatile("s_waitcnt vmcnt(%0)" ::"i"(n) : "memory")
#define WAIT_L0() do { asm volatile("" ::: "memory"); __builtin_amdgcn_s_waitcnt(0xC07F); asm volatile("" ::: "memory"); } while (0)
#define BAR() asm volatile("s_waitcnt lgkmcnt(0)\n\ts_barrier" ::: "memory")
typedef __attribute__((address_space(1))) unsigned long long gu64;
typedef __attribute__((address_space(1))) unsigned gu32;
DEVI void st_gran(char* p, unsigned tag, unsigned v) {
  __hip_atomic_store((gu64*)p, ((unsigned long long)tag << 32) | v, __ATOMIC_RELAXED, __HIP_MEMORY_SCOPE_AGENT);
}
template <int N>
DEVI void sweep(const char* p, int S, unsigned tag, unsigned (&v)[N]) {
  for (int spins = 0; spins < (1 << 16); ++spins) {
    bool ok = true;
#pragma unroll
    for (int k = 0; k < N; ++k) {
      const unsigned long long x = __hip_atomic_load((gu64*)(p + k * S), __ATOMIC_RELAXED, __HIP_MEMORY_SCOPE_AGENT);
      v[k] = (unsigned)x;
      ok &= (unsigned)(x >> 32) == tag;
    }
    if (__all(ok)) break;
    __builtin_amdgcn_s_sleep(8);
  }
}
#define SCHED() __builtin_amdgcn_sched_barrier(0)
DEVI f32x2 halves(float v) {
  const unsigned u = __builtin_bit_cast(unsigned, v);
  auto r = __builtin_amdgcn_permlane32_swap(u, u, false, false);
  const unsigned r0 = r[0], r1 = r[1];
  return f32x2{__builtin_bit_cast(float, r0), __builtin_bit_cast(float, r1)};
}

struct Strm { const char* base; int ld; unsigned voff; };
DEVI Strm mk_strm(const char* base, int ld, int lane) {
  return Strm{base, ld, (unsigned)((lane >> 3) * ld + ((lane & 7) ^ (((lane >> 4) & 3) << 1)) * 16)};
}
DEVI void issue_slab(unsigned slot, const Strm& s, int ks) {
  const char* rb = s.base + (size_t)(ks * 32 * s.ld);
#pragma unroll
  for (int i = 0; i < 4; ++i) dma16(slot + i * 1024, s.voff, rb + (size_t)(i * 8 * s.ld));
}
struct Frag { u32x4 w[4], x[4]; };
template <bool MK>
DEVI void read_frag(Frag& f, const unsigned (&ta)[4], unsigned soff, unsigned xaddr, int tm) {
#pragma unroll
  for (int t = 0; t < 4; ++t)
    if (!MK || (tm >> t & 1)) {
      const u32x2 lo = trd(ta[t] + soff), hi = trd(ta[t] + soff + 2048);
      f.w[t] = u32x4{lo.x, lo.y, hi.x, hi.y};
    }
#pragma unroll
  for (int t = 0; t < 4; ++t) f.x[t] = lrd128(xaddr + t * 1024);
}
template <bool MK, int T0, int T1>
DEVI void mma_part(f32x4 (&acc)[4][4], const Frag& f, int tm) {
#pragma unroll
  for (int t = T0; t < T1; ++t)
    if (!MK || (tm >> t & 1)) {
#pragma unroll
      for (int m = 0; m < 4; ++m) acc[t][m] = mma16(f.w[t], f.x[m], acc[t][m]);
    }
}
DEVI int wrap18(int k) { return k >= 18 ? k - 18 : k; }
template <bool MK>
DEVI void gemm(f32x4 (&acc)[4][4], const unsigned (&ta)[4], unsigned xr0, unsigned xr1, unsigned slot0, const Strm& cur, int k0, int n,
               bool has_next, const Strm& nx, int nk0, int tm = 15) {
  Frag f0, f1;
  if (MK) {
#pragma unroll
    for (int t = 0; t < 4; ++t) f0.w[t] = f1.w[t] = u32x4{0u, 0u, 0u, 0u};
  }
  WAIT_V(4);
  read_frag<MK>(f0, ta, 0, xr0 + k0 * 4096, tm);
  WAIT_L0();
  int k = k0;
  issue_slab(slot0, cur, wrap18(k + 2));
  for (int it = 2; it < n; it += 2) {
    const int k2 = wrap18(k + 2);
    WAIT_V(4);
    read_frag<MK>(f1, ta, 4096, xr1 + k * 4096, tm);
    SCHED();
    mma_part<MK, 0, 2>(acc, f0, tm);
    SCHED();
    WAIT_L0();
    issue_slab(slot0 + 4096, cur, k2 + 1);
    SCHED();
    mma_part<MK, 2, 4>(acc, f0, tm);
    SCHED();
    WAIT_V(4);
    read_frag<MK>(f0, ta, 0, xr0 + k2 * 4096, tm);
    SCHED();
    mma_part<MK, 0, 2>(acc, f1, tm);
    SCHED();
    WAIT_L0();
    if (it + 2 < n) issue_slab(slot0, cur, wrap18(k2 + 2));
    else if (has_next) issue_slab(slot0, nx, nk0);
    SCHED();
    mma_part<MK, 2, 4>(acc, f1, tm);
    SCHED();
    k = k2;
  }
  if (has_next) WAIT_V(4); else WAIT_V(0);
  read_frag<MK>(f1, ta, 4096, xr1 + k * 4096, tm);
  SCHED();
  mma_part<MK, 0, 2>(acc, f0, tm);
  SCHED();
  WAIT_L0();
  if (has_next) issue_slab(slot0 + 4096, nx, nk0 + 1);
  SCHED();
  mma_part<MK, 2, 4>(acc, f0, tm);
  mma_part<MK, 0, 4>(acc, f1, tm);
}

DEVI float mish(float h) {
  float e = __builtin_amdgcn_exp2f(h * 1.4426950408889634f);
  float d = __builtin_fmaf(e, e + 2.0f, 2.0f);
  float r = __builtin_amdgcn_rcpf(d);
  return h * __builtin_fmaf(-2.0f, r, 1.0f);
}

DEVI void set_bias(f32x4 (&acc)[4][4], const u32x2 (&b)[4]) {
#pragma unroll
  for (int t = 0; t < 4; ++t)
#pragma unroll
    for (int m = 0; m < 4; ++m) acc[t][m] = f32x4{bflo(b[t].x), bfhi(b[t].x), bflo(b[t].y), bfhi(b[t].y)};
}
DEVI void set_zero(f32x4 (&acc)[4][4]) {
#pragma unroll
  for (int t = 0; t < 4; ++t)
#pragma unroll
    for (int m = 0; m < 4; ++m) acc[t][m] = f32x4{0.f, 0.f, 0.f, 0.f};
}
template <int H0 = 0, int H1 = 2>
DEVI void mish_pack(const f32x4 (&acc)[4][4], u32x4 (&act)[2][4]) {
#pragma unroll
  for (int h = H0; h < H1; ++h)
#pragma unroll
    for (int m = 0; m < 4; ++m) {
      act[h][m] = u32x4{pk2(mish(acc[2 * h][m][0]), mish(acc[2 * h][m][1])), pk2(mish(acc[2 * h][m][2]), mish(acc[2 * h][m][3])),
                        pk2(mish(acc[2 * h + 1][m][0]), mish(acc[2 * h + 1][m][1])), pk2(mish(acc[2 * h + 1][m][2]), mish(acc[2 * h + 1][m][3]))};
      asm volatile("" : "+v"(act[h][m]));
    }
}
DEVI void write_act(const u32x4 (&act)[2][4], unsigned xr0, unsigned xr1, int blk) {
#pragma unroll
  for (int m = 0; m < 4; ++m) { lwr128(xr0 + blk * 8192 + m * 1024, act[0][m]); lwr128(xr1 + blk * 8192 + m * 1024, act[1][m]); }
}
DEVI void pack_part(const f32x4 (&acc)[4][4], u32x4 (&o)[8]) {
#pragma unroll
  for (int t = 0; t < 4; ++t)
#pragma unroll
    for (int h = 0; h < 2; ++h) {
      o[t * 2 + h] = u32x4{pk2(acc[t][2 * h][0], acc[t][2 * h][1]), pk2(acc[t][2 * h][2], acc[t][2 * h][3]),
                           pk2(acc[t][2 * h + 1][0], acc[t][2 * h + 1][1]), pk2(acc[t][2 * h + 1][2], acc[t][2 * h + 1][3])};
      asm volatile("" : "+v"(o[t * 2 + h]));
    }
}
DEVI void store_part(const u32x4 (&o)[8], char* P, int blk) {
#pragma unroll
  for (int e = 0; e < 8; ++e) __builtin_nontemporal_store(o[e], (u32x4*)(P + ((e & 1) * 36 + blk * 4 + (e >> 1)) * 1024));
}

DEVI void hedgehog(const f32x16& p, unsigned (&out)[10]) {
  float mx = p[0], mn = p[0];
#pragma unroll
  for (int r = 1; r < 9; ++r) { mx = fmaxf(mx, p[r]); mn = fminf(mn, p[r]); }
  { const f32x2 a = halves(mx), b = halves(mn); mx = fmaxf(a.x, a.y); mn = fminf(b.x, b.y); }
  const float L2E = 1.4426950408889634f;
  const float mxl = mx * L2E, mnl = mn * L2E;
  float e[18], sp = 0.f, sn = 0.f;
#pragma unroll
  for (int r = 0; r < 9; ++r) {
    e[r] = __builtin_amdgcn_exp2f(__builtin_fmaf(p[r], L2E, -mxl));
    e[9 + r] = __builtin_amdgcn_exp2f(__builtin_fmaf(p[r], -L2E, mnl));
    sp += e[r]; sn += e[9 + r];
  }
  { const f32x2 a = halves(sp), b = halves(sn); sp = a.x + a.y; sn = b.x + b.y; }
  const float ip = __builtin_amdgcn_rcpf(sp), in = __builtin_amdgcn_rcpf(sn);
#pragma unroll
  for (int r = 0; r < 9; ++r) { e[r] *= ip; e[9 + r] *= in; }
#pragma unroll
  for (int r = 0; r < 9; ++r) out[r] = pk2(e[2 * r], e[2 * r + 1]);
  out[9] = 0u;
}

__global__ __launch_bounds__(512, 2) void k1(const u16* __restrict__ x, const u16* __restrict__ wqkv, const u16* __restrict__ wphi,
                                             const u16* __restrict__ wo, const u16* __restrict__ w1, const u16* __restrict__ b1,
                                             const u16* __restrict__ w2, char* __restrict__ ws) {
  __shared__ __attribute__((aligned(16))) char smem[LDS_BYTES];
  asm volatile("" ::"v"((unsigned)(uintptr_t)(LDS_AS char*)smem));
  const int tid = threadIdx.x, lane = tid & 63, w = __builtin_amdgcn_readfirstlane(tid >> 6);
  const int j = (blockIdx.x & 6) | ((blockIdx.x >> 3) & 1), i = (blockIdx.x >> 4) | ((blockIdx.x & 1) << 4);
  const int c16 = lane & 15, g = lane >> 4;
  const unsigned slot0 = RING_OFF + w * 8192;
  STAMP_DECL;
  STAMP(0);
  const int r8 = lane >> 3, sub = lane & 7;
  const int mrow = w * 8 + ((r8 >> 1) | ((r8 & 1) << 2));
  u32x4 xv[9];
  {
    const unsigned vo = mrow * 1152 + sub * 16;
    const char* xb = (const char*)x + (size_t)i * 64 * 1152;
#pragma unroll
    for (int c = 0; c < 9; ++c) asm volatile("global_load_dwordx4 %0, %1, %2 offset:%3" : "=v"(xv[c]) : "v"(vo), "s"(xb), "i"(c * 128) : "memory");
  }
  u32x2 bia[4], bi8[4];
  {
    const char* bb = (const char*)b1 + j * 1152;
    const unsigned vo = w * 128 + g * 8, v8 = 1024 + g * 8;
#pragma unroll
    for (int t = 0; t < 4; ++t) {
      asm volatile("global_load_dwordx2 %0, %1, %2 offset:%3" : "=v"(bia[t]) : "v"(vo), "s"(bb), "i"(t * 32) : "memory");
      asm volatile("global_load_dwordx2 %0, %1, %2 offset:%3" : "=v"(bi8[t]) : "v"(v8), "s"(bb), "i"(t * 32) : "memory");
    }
  }
  unsigned tagv;
  asm volatile("global_load_dword %0, %1, %2 sc1" : "=v"(tagv) : "v"(j * 128), "s"(ws + WS_EP) : "memory");
  asm volatile("s_barrier" ::: "memory");
  const int rot = 2 * (i % 9);
  const int part = (w - (w >> 2)) >> 1, line = (w - (w >> 2)) & 1;
  const Strm s1 = mk_strm((const char*)w1 + j * 1152 + w * 128, FF * 2, lane);
  const Strm sr = w == 3 ? mk_strm((const char*)w1 + j * 1152 + 1024, FF * 2, lane)
                         : mk_strm((const char*)wqkv + (part == 0 ? 2304 : (part - 1) * 1152) + (j + line) * 128, 3456, lane);
  if (w < 7) {
    issue_slab(slot0, sr, rot);
    issue_slab(slot0 + 4096, sr, rot + 1);
  } else {
#pragma unroll
    for (int s = 0; s < 3; ++s) { int c = s * 64 + lane; c = c > 161 ? 161 : c; dma16(WPHI_OFF + s * 1024, c * 16, (const char*)wphi + j * 2592); }
#pragma unroll
    for (int s = 0; s < 5; ++s) dma16(DUMMY_OFF, (j * 576 + (i & 15) * 36) * 1152 + (s * 64 + lane) * 128, (const char*)w2);
  }
  STAMP(1);
  asm volatile("s_waitcnt vmcnt(8)" : "+v"(xv[0]), "+v"(xv[1]), "+v"(xv[2]), "+v"(xv[3]), "+v"(xv[4]), "+v"(xv[5]), "+v"(xv[6]), "+v"(xv[7]), "+v"(xv[8]),
               "+v"(bia[0]), "+v"(bia[1]), "+v"(bia[2]), "+v"(bia[3]), "+v"(bi8[0]), "+v"(bi8[1]), "+v"(bi8[2]), "+v"(bi8[3]), "+v"(tagv)::"memory");
  const unsigned tag = __builtin_amdgcn_readfirstlane(tagv) + 1u;
  {
    STAMP(2);
    float s = 0.f, v = 0.f;
    const bf16x2 one2 = __builtin_bit_cast(bf16x2, 0x3f803f80u);
#pragma unroll
    for (int c = 0; c < 9; ++c)
#pragma unroll
      for (int e = 0; e < 4; ++e) {
        const unsigned xu = xv[c][e];
        const bf16x2 xx = __builtin_bit_cast(bf16x2, xu);
        s = __builtin_amdgcn_fdot2_f32_bf16(xx, one2, s, false);
        v = __builtin_amdgcn_fdot2_f32_bf16(xx, xx, v, false);
      }
    s += __shfl_xor(s, 1); v += __shfl_xor(v, 1); s += __shfl_xor(s, 2); v += __shfl_xor(v, 2); s += __shfl_xor(s, 4); v += __shfl_xor(v, 4);
    const float mean = s * (1.0f / 576.0f);
    const float var = fmaxf(__builtin_fmaf(-mean, mean, v * (1.0f / 576.0f)), 0.f);
    const float rs = __builtin_amdgcn_rsqf(var + 1e-5f), nb = -mean * rs;
    const int f = ((sub >> 2) << 1) + (sub & 1);
    const unsigned wa = XA_OFF + (sub >> 2) * 4096 + (mrow >> 4) * 1024 + ((sub & 1) * 32 + ((mrow & 15) ^ f)) * 16 + ((sub >> 1) & 1) * 8;
#pragma unroll
    for (int c = 0; c < 9; ++c) {
      unsigned o[4];
#pragma unroll
      for (int e = 0; e < 4; ++e) o[e] = pk2(__builtin_fmaf(bflo(xv[c][e]), rs, nb), __builtin_fmaf(bfhi(xv[c][e]), rs, nb));
      lwr64(wa + c * 8192, u32x2{o[0], o[1]});
      lwr64(wa + c * 8192 + 256, u32x2{o[2], o[3]});
    }
  }
  STAMP(3);
  BAR();
  STAMP(4);
  const unsigned xr0 = XA_OFF + (g * 16 + (c16 ^ (g >> 1))) * 16, xr1 = XA_OFF + 4096 + (g * 16 + (c16 ^ (2 + (g >> 1)))) * 16;
  unsigned ta[4];
  {
    const int q = (lane >> 2) & 3, p = lane & 3, r = 4 * g + q, s = (r >> 1) & 3;
#pragma unroll
    for (int t = 0; t < 4; ++t) ta[t] = slot0 + 128 * r + 32 * (t ^ s) + 8 * p;
  }
  f32x4 acc[4][4];
  if (w == 3) set_bias(acc, bi8); else set_zero(acc);
  if (w < 7) {
    const int tm = w == 3 ? 15 : (line == 0 ? (15 << (j >> 1)) & 15 : 15 >> (3 - (j >> 1)));
    gemm<true>(acc, ta, xr0, xr1, slot0, sr, rot, 18, w < 4, s1, rot, tm);
  } else {
    dma16(DUMMY_OFF, (j * 576 + (i & 15) * 36) * 1152 + (320 + (lane & 3)) * 128, (const char*)w2);
#pragma unroll
    for (int s = 0; s < 3; ++s) {
      const int fr = blockIdx.x + s * 256;
      if (fr < 648) {
        const int ks = fr / 36, nt = fr - ks * 36;
        const int ch = ks * 4 + g, c9 = ch % 9;
        const u16* src = wo + ((ch / 9) * 72 + (c9 < 8 ? 9 * c9 : 8)) * 576 + nt * 16 + c16;
        const int st = c9 < 8 ? 576 : 9 * 576;
        u32x4 o;
#pragma unroll
        for (int e = 0; e < 4; ++e) o[e] = (unsigned)src[(2 * e) * st] | ((unsigned)src[(2 * e + 1) * st] << 16);
        *(u32x4*)(ws + WS_WOT + ((size_t)fr * 64 + lane) * 16) = o;
      }
    }
    WAIT_V(0);
    const int r32 = lane & 31, reg = (r32 & 3) + 4 * (r32 >> 3), f = 9 * ((r32 >> 2) & 1) + reg;
#pragma nounroll
    for (int u = 0; u < 8; ++u) {
      const int hh = u >> 1;
      u32x4 wp;
#pragma unroll
      for (int e = 0; e < 4; ++e) {
        unsigned v2[2];
#pragma unroll
        for (int b = 0; b < 2; ++b) {
          const int d = 16 * (u & 1) + 8 * (lane >> 5) + 2 * e + b - 2 * hh;
          const bool ok = reg < 9 && d >= 0 && d < 18;
          const unsigned val = *(LDS_AS u16*)(uintptr_t)(WPHI_OFF + (ok ? (hh * 324 + d * 18 + f) * 2 : 0));
          v2[b] = ok ? val : 0u;
        }
        wp[e] = v2[0] | (v2[1] << 16);
      }
      lwr128(PH_OFF + hh * 5120 + (u & 1) * 1024 + lane * 16, wp);
    }
  }
  u32x4 act8[2][4];
  STAMP(5);
  BAR();
  if (w == 7) {
    lwr128(S2_OFF + lane * S2_ROW + 432, u32x4{0u, 0u, 0u, 0u});
    *(LDS_AS unsigned*)(uintptr_t)(WPHI_OFF + 3068) = 0u;
  } else if (w != 3) {
#pragma unroll
    for (int t = 0; t < 4; ++t) {
      const int lc = 64 * line + 16 * t + 4 * g - 8 * j;
      if (lc >= 0 && lc < 72) {
#pragma unroll
        for (int m = 0; m < 4; ++m)
          lwr64(S2_OFF + (m * 16 + c16) * S2_ROW + part * 144 + lc * 2, u32x2{pk2(acc[t][m][0], acc[t][m][1]), pk2(acc[t][m][2], acc[t][m][3])});
      }
    }
  } else {
    mish_pack<0, 1>(acc, act8);
  }
  BAR();
  STAMP(6);
  const Strm s2 = mk_strm((const char*)w2 + (size_t)j * 576 * 1152 + w * 128, DM * 2, lane);
  char* P = ws + WS_P + ((size_t)(j * 64 + 2 * i) * 36 * 64 + lane) * 16;
  u32x4 act[2][4];
  if (w < 4) {
    if (w == 3) mish_pack<1, 2>(acc, act8);
    set_bias(acc, bia);
    gemm<false>(acc, ta, xr0, xr1, slot0, s1, rot, 18, true, s2, 16);
    STAMP(7);
    mish_pack(acc, act);
    STAMP(8);
    BAR();
    write_act(act, xr0, xr1, w);
    if (w == 3) write_act(act8, xr0, xr1, 8);
    BAR();
    STAMP(9);
    set_zero(acc);
    gemm<false>(acc, ta, xr0, xr1, slot0, s2, 16, 10, true, s2, 8);
    STAMP(10);
    BAR();
    if (w != 3) {
      const int col = i * 48 + w * 16 + c16;
      unsigned pv[8];
      sweep<8>(ws + WS_KVP + ((size_t)(j * 32 + g * 8) * 1536 + (col < 1520 ? col : 0)) * 8, 1536 * 8, tag, pv);
      float a = 0.f, b = 0.f;
#pragma unroll
      for (int e = 0; e < 8; ++e) { a += bflo(pv[e]); b += bfhi(pv[e]); }
      a += __shfl_xor(a, 16); b += __shfl_xor(b, 16); a += __shfl_xor(a, 32); b += __shfl_xor(b, 32);
      if (g == 0) st_gran(ws + WS_KV + (size_t)(j * 1536 + col) * 8, tag, pk2(a, b));
      if (i == 0 && w == 1 && lane == 0) __hip_atomic_store((gu32*)(ws + WS_EP + j * 128), tag, __ATOMIC_RELAXED, __HIP_MEMORY_SCOPE_AGENT);
    }
    const Strm s8 = mk_strm((const char*)w2 + (size_t)j * 576 * 1152 + 1024, DM * 2, lane);
    gemm<false>(acc, ta, xr0, xr1, slot0, s2, 8, 8, true, s8, 4 * w);
    STAMP(11);
    u32x4 po[8], p8[8];
    pack_part(acc, po);
    set_zero(acc);
    gemm<false>(acc, ta, xr0, xr1, slot0, s8, 4 * w, w == 3 ? 6 : 4, false, s8, 0);
    store_part(po, P, w);
    pack_part(acc, p8);
#pragma unroll
    for (int e = 0; e < 8; ++e) lwr128(slot0 + e * 1024 + lane * 16, p8[e]);
    {
      const int r32 = lane & 31, hf = lane >> 5, reg = (r32 & 3) + 4 * (r32 >> 3);
      const int d = reg < 9 ? 9 * ((r32 >> 2) & 1) + reg : 18;
      unsigned kq[10], fq[2][10];
#pragma unroll
      for (int mh = 0; mh < 2; ++mh) {
        f32x16 p;
#pragma unroll
        for (int e = 0; e < 5; ++e) {
          const u32x2 u = lrd64(PH_OFF + w * 5120 + (mh * 32 + r32) * 80 + hf * 40 + e * 8);
          const unsigned a = u.x, b = u.y;
          p[2 * e] = __uint_as_float(a); p[2 * e + 1] = __uint_as_float(b);
        }
        hedgehog(p, fq[mh]);
      }
      sweep<10>(ws + WS_KV + (size_t)(j * 1536 + w * 380 + d * 20 + hf * 10) * 8, 8, tag, kq);
      const int ch = 9 * j + 2 * w + hf, c8 = 9 * j + 8;
#pragma unroll
      for (int mh = 0; mh < 2; ++mh) {
        f32x16 o = {0.f, 0.f, 0.f, 0.f, 0.f, 0.f, 0.f, 0.f, 0.f, 0.f, 0.f, 0.f, 0.f, 0.f, 0.f, 0.f};
        o = mma32(u32x4{kq[0], kq[1], kq[2], kq[3]}, u32x4{fq[mh][0], fq[mh][1], fq[mh][2], fq[mh][3]}, o);
        o = mma32(u32x4{kq[4], kq[5], kq[6], kq[7]}, u32x4{fq[mh][4], fq[mh][5], fq[mh][6], fq[mh][7]}, o);
        o = mma32(u32x4{kq[8], kq[9], 0u, 0u}, u32x4{fq[mh][8], fq[mh][9], 0u, 0u}, o);
        const float inv = 1.0f / (o[9] + 1e-6f);
        char* ob = ws + WS_O + ((size_t)(2 * i + mh) * 36 + (r32 >> 4)) * 1024 + (r32 & 15) * 16;
        *(u32x4*)(ob + (ch >> 2) * 2048 + (ch & 3) * 256) =
            u32x4{pk2(o[0] * inv, o[1] * inv), pk2(o[2] * inv, o[3] * inv), pk2(o[4] * inv, o[5] * inv), pk2(o[6] * inv, o[7] * inv)};
        *(u16*)(ob + (c8 >> 2) * 2048 + (c8 & 3) * 256 + (2 * w + hf) * 2) = (u16)pk2(o[8] * inv, 0.f);
      }
    }
  } else {
    const int hh = w & 3, r32 = lane & 31, hf = lane >> 5;
    __builtin_amdgcn_s_setprio(2);
    const u32x4 wp[2] = {lrd128(PH_OFF + hh * 5120 + lane * 16), lrd128(PH_OFF + hh * 5120 + 1024 + lane * 16)};
    f32x16 pk[2], pq[2];
    u32x4 vf[2][2];
    const f32x16 z16 = {0.f, 0.f, 0.f, 0.f, 0.f, 0.f, 0.f, 0.f, 0.f, 0.f, 0.f, 0.f, 0.f, 0.f, 0.f, 0.f};
#pragma unroll
    for (int mh = 0; mh < 2; ++mh) {
      const unsigned rowa = S2_OFF + (mh * 32 + r32) * S2_ROW + hh * 32 + hf * 16;
      pk[mh] = mma32(wp[1], lrd128(rowa + 288 + 32), mma32(wp[0], lrd128(rowa + 288), z16));
      pq[mh] = mma32(wp[1], lrd128(rowa + 144 + 32), mma32(wp[0], lrd128(rowa + 144), z16));
      const unsigned va = S2_OFF + (mh * 32 + 8 * g + ((lane >> 2) & 3)) * S2_ROW + 8 * (lane & 3) + hh * 32;
#pragma unroll
      for (int dt = 0; dt < 2; ++dt) {
        const u32x2 v0 = trd(va + dt * 32), v1 = trd(va + 4 * S2_ROW + dt * 32);
        vf[mh][dt] = u32x4{v0.x, v0.y, v1.x, v1.y};
      }
    }
    WAIT_L0();
    LDS_AS unsigned* cnt = (LDS_AS unsigned*)(uintptr_t)(WPHI_OFF + 3068);
    if (lane == 0) __hip_atomic_fetch_add(cnt, 1u, __ATOMIC_RELAXED, __HIP_MEMORY_SCOPE_WORKGROUP);
#pragma unroll
    for (int mh = 0; mh < 2; ++mh) {
      unsigned ph[10];
      hedgehog(pk[mh], ph);
#pragma unroll
      for (int e = 0; e < 5; ++e) lwr64(PH_OFF + hh * 5120 + (mh * 32 + r32) * 80 + hf * 40 + e * 8, u32x2{ph[2 * e], ph[2 * e + 1]});
    }
    while (__builtin_amdgcn_readfirstlane(__hip_atomic_load(cnt, __ATOMIC_RELAXED, __HIP_MEMORY_SCOPE_WORKGROUP)) < 4u) __builtin_amdgcn_s_sleep(1);
    issue_slab(slot0, s1, rot);
    issue_slab(slot0 + 4096, s1, rot + 1);
    {
      const int q = (lane >> 2) & 3, p = lane & 3;
      const unsigned pa = PH_OFF + hh * 5120 + (8 * g + q) * 80 + 8 * p;
      f32x4 kv[2][3];
#pragma unroll
      for (int e = 0; e < 6; ++e) kv[e / 3][e % 3] = f32x4{0.f, 0.f, 0.f, 0.f};
#pragma unroll
      for (int s = 0; s < 2; ++s) {
        if (c16 == 15) vf[s][1] = u32x4{0x3f803f80u, 0x3f803f80u, 0x3f803f80u, 0x3f803f80u};
#pragma unroll
        for (int ft = 0; ft < 3; ++ft) {
          const u32x2 p0 = trd(pa + s * 32 * 80 + ft * 32), p1 = trd(pa + (s * 32 + 4) * 80 + ft * 32);
          const u32x4 pf = {p0.x, p0.y, p1.x, p1.y};
          kv[0][ft] = mma16(pf, vf[s][0], kv[0][ft]);
          kv[1][ft] = mma16(pf, vf[s][1], kv[1][ft]);
        }
      }
#pragma unroll
      for (int dt = 0; dt < 2; ++dt) {
        const int col = dt * 16 + c16, d = col == 31 ? 18 : col - 2 * hh;
        if (d >= 0 && (d < 18 || col == 31)) {
          char* dst = ws + WS_KVP + ((size_t)(j * 32 + i) * 1536 + hh * 380 + d * 20 + 2 * g) * 8;
#pragma unroll
          for (int ft = 0; ft < 3; ++ft)
            if (ft < 2 || g < 2) {
              const u32x4 gr = {pk2(kv[dt][ft][0], kv[dt][ft][1]), tag, pk2(kv[dt][ft][2], kv[dt][ft][3]), tag};
              asm volatile("global_store_dwordx4 %0, %1, off offset:%2 sc1\n\ts_nop 1" ::"v"(dst), "v"(gr), "i"(ft * 64) : "memory");
            }
        }
      }
#pragma unroll
      for (int mh = 0; mh < 2; ++mh)
#pragma unroll
        for (int e = 0; e < 5; ++e) {
          const float a = pq[mh][2 * e], b = pq[mh][2 * e + 1];
          lwr64(PH_OFF + hh * 5120 + (mh * 32 + r32) * 80 + hf * 40 + e * 8, u32x2{__float_as_uint(a), __float_as_uint(b)});
        }
    }
    STAMP(7);
    __builtin_amdgcn_s_setprio(0);
    set_bias(acc, bia);
    gemm<false>(acc, ta, xr0, xr1, slot0, s1, rot, 18, true, s2, 0);
    STAMP(8);
    BAR();
    BAR();
    STAMP(9);
    mish_pack(acc, act);
    write_act(act, xr0, xr1, w);
    STAMP(10);
    BAR();
    set_zero(acc);
    gemm<false>(acc, ta, xr0, xr1, slot0, s2, 0, 18, false, s2, 0);
    STAMP(11);
    u32x4 po[8];
    pack_part(acc, po);
    store_part(po, P, w);
  }
  STAMP(12);
  BAR();
  {
    float v[8] = {0.f, 0.f, 0.f, 0.f, 0.f, 0.f, 0.f, 0.f};
#pragma unroll
    for (int a = 0; a < 4; ++a) {
      const u32x4 u = lrd128(RING_OFF + a * 8192 + w * 1024 + lane * 16);
#pragma unroll
      for (int e = 0; e < 4; ++e) { v[2 * e] += bflo(u[e]); v[2 * e + 1] += bfhi(u[e]); }
    }
    *(u32x4*)(P + ((w & 1) * 36 + 32 + (w >> 1)) * 1024) = u32x4{pk2(v[0], v[1]), pk2(v[2], v[3]), pk2(v[4], v[5]), pk2(v[6], v[7])};
  }
  STAMP(13);
  STAMP(14); STAMP(15);
  STAMP_FLUSH(ws);
}

__global__ __launch_bounds__(576) void k2(const u16* __restrict__ x, const char* __restrict__ ws, u16* __restrict__ out) {
  __shared__ __attribute__((aligned(16))) char ob[36864];
  const int tid = threadIdx.x, lane = tid & 63, w = __builtin_amdgcn_readfirstlane(tid >> 6);
  const int b = blockIdx.x, i32 = (b & 7) + 8 * (b >> 5), nt = ((b >> 3) & 3) * 9 + w;
  const int c16 = lane & 15, g = lane >> 4;
  const unsigned obase = (unsigned)(uintptr_t)(LDS_AS char*)ob;
#pragma unroll
  for (int s = 0; s < 4; ++s) dma16(obase + (w * 4 + s) * 1024, lane * 16, ws + WS_O + ((size_t)i32 * 36 + w * 4 + s) * 1024);
  u32x4 wf[18], pp[8];
  u32x2 xr[2];
#pragma unroll
  for (int s = 0; s < 8; ++s) pp[s] = *(const u32x4*)(ws + WS_P + ((size_t)((s * 64 + i32) * 36 + nt) * 64 + lane) * 16);
  asm volatile("" ::: "memory");
#pragma unroll
  for (int ks = 0; ks < 18; ++ks) wf[ks] = *(const u32x4*)(ws + WS_WOT + ((size_t)(ks * 36 + nt) * 64 + lane) * 16);
#pragma unroll
  for (int m = 0; m < 2; ++m) xr[m] = *(const u32x2*)(x + (size_t)(i32 * 32 + m * 16 + c16) * 576 + nt * 16 + g * 4);
  WAIT_V(28);
  BAR();
  u32x4 of[12];
#pragma unroll
  for (int e = 0; e < 12; ++e) of[e] = lrd128(obase + e * 1024 + lane * 16);
  SCHED();
  float gt[2][4];
#pragma unroll
  for (int m = 0; m < 2; ++m) {
    float bs[4] = {0.f, 0.f, 0.f, 0.f};
#pragma unroll
    for (int s = 0; s < 8; ++s) {
      bs[0] += bflo(pp[s][2 * m]); bs[1] += bfhi(pp[s][2 * m]); bs[2] += bflo(pp[s][2 * m + 1]); bs[3] += bfhi(pp[s][2 * m + 1]);
    }
#pragma unroll
    for (int r = 0; r < 4; ++r) gt[m][r] = bs[r] * __builtin_amdgcn_rcpf(1.0f + __builtin_amdgcn_exp2f(-1.4426950408889634f * bs[r]));
    asm volatile("" : "+v"(gt[m][0]), "+v"(gt[m][1]), "+v"(gt[m][2]), "+v"(gt[m][3]));
  }
  SCHED();
  f32x4 acc[2] = {{0.f, 0.f, 0.f, 0.f}, {0.f, 0.f, 0.f, 0.f}};
#pragma unroll
  for (int e = 0; e < 36; ++e) {
    acc[e & 1] = mma16(wf[e >> 1], of[e % 12], acc[e & 1]);
    if (e < 24) of[e % 12] = lrd128(obase + (e + 12) * 1024 + lane * 16);
    SCHED();
  }
#pragma unroll
  for (int m = 0; m < 2; ++m) {
    const float xs[4] = {bflo(xr[m].x), bfhi(xr[m].x), bflo(xr[m].y), bfhi(xr[m].y)};
    float o[4];
#pragma unroll
    for (int r = 0; r < 4; ++r) o[r] = __builtin_fmaf(acc[m][r], gt[m][r], xs[r]);
    *(u32x2*)(out + (size_t)(i32 * 32 + m * 16 + c16) * 576 + nt * 16 + g * 4) = u32x2{pk2(o[0], o[1]), pk2(o[2], o[3])};
  }
}

extern "C" void kernel_launch(void* const* d_in, const int* in_sizes, int n_in, void* d_out, int out_size, void* d_ws, size_t ws_size,
                              hipStream_t stream) {
  const u16* x = (const u16*)d_in[0];
  const u16* wqkv = (const u16*)d_in[1];
  const u16* wphi = (const u16*)d_in[2];
  const u16* wo = (const u16*)d_in[3];
  const u16* w1 = (const u16*)d_in[5];
  const u16* b1 = (const u16*)d_in[6];
  const u16* w2 = (const u16*)d_in[7];
  char* ws = (char*)d_ws;
  k1<<<256, 512, 0, stream>>>(x, wqkv, wphi, wo, w1, b1, w2, ws);
  k2<<<256, 576, 0, stream>>>(x, ws, (u16*)d_out);
}
```

```cpp
#include <hip/hip_runtime.h>
#include <hip/hip_bf16.h>
#include <stdint.h>


typedef __attribute__((ext_vector_type(8))) __bf16 bf16x8;
typedef __attribute__((ext_vector_type(2))) __bf16 bf16x2;
typedef __attribute__((ext_vector_type(4))) short s16x4;
typedef __attribute__((ext_vector_type(2))) float f32x2;
typedef __attribute__((ext_vector_type(4))) float f32x4;
typedef __attribute__((ext_vector_type(16))) float f32x16;
typedef __attribute__((ext_vector_type(4))) unsigned u32x4;
typedef __attribute__((ext_vector_type(2))) unsigned u32x2;
typedef unsigned short u16;
#define LDS_AS __attribute__((address_space(3)))
#define DEVI __device__ __forceinline__

constexpr int SEQ = 2048, DM = 576, FF = 4608;
constexpr int XA_OFF = 0;
constexpr int RING_OFF = 73728;
constexpr int S2_OFF = RING_OFF + 32768;
constexpr int S2_ROW = 464;
constexpr int WPHI_OFF = RING_OFF + 65536;
constexpr int PH_OFF = WPHI_OFF + 3072;
constexpr int DUMMY_OFF = PH_OFF + 20480;
constexpr int LDS_BYTES = 163840;
constexpr size_t WS_P = 0;
constexpr size_t WS_KVP = WS_P + (size_t)8 * 64 * 36 * 1024;
constexpr size_t WS_KV = WS_KVP + (size_t)8 * 32 * 1536 * 8;
constexpr size_t WS_EP = WS_KV + (size_t)8 * 1536 * 8;
constexpr size_t WS_O = WS_EP + 1024;
constexpr size_t WS_WOT = WS_O + (size_t)64 * 36 * 1024;

DEVI unsigned pk2(float a, float b) {
  f32x2 v = {a, b};
  return __builtin_bit_cast(unsigned, __builtin_convertvector(v, bf16x2));
}
DEVI float bflo(unsigned u) { return __builtin_bit_cast(float, u << 16); }
DEVI float bfhi(unsigned u) { return __builtin_bit_cast(float, u & 0xffff0000u); }
DEVI u32x2 trd(unsigned a) {
  return __builtin_bit_cast(u32x2, __builtin_amdgcn_ds_read_tr16_b64_v4i16((LDS_AS s16x4*)(uintptr_t)a));
}
DEVI u32x4 lrd128(unsigned a) { return *(LDS_AS u32x4*)(uintptr_t)a; }
DEVI u32x2 lrd64(unsigned a) { return *(LDS_AS u32x2*)(uintptr_t)a; }
DEVI void lwr128(unsigned a, u32x4 v) { *(LDS_AS u32x4*)(uintptr_t)a = v; }
DEVI void lwr64(unsigned a, u32x2 v) { *(LDS_AS u32x2*)(uintptr_t)a = v; }
DEVI f32x4 mma16(u32x4 a, u32x4 b, f32x4 c) {
  return __builtin_amdgcn_mfma_f32_16x16x32_bf16(__builtin_bit_cast(bf16x8, a), __builtin_bit_cast(bf16x8, b), c, 0, 0, 0);
}
DEVI f32x16 mma32(u32x4 a, u32x4 b, f32x16 c) {
  return __builtin_amdgcn_mfma_f32_32x32x16_bf16(__builtin_bit_cast(bf16x8, a), __builtin_bit_cast(bf16x8, b), c, 0, 0, 0);
}
DEVI void dma16(unsigned ldsb, unsigned voff, const char* base) {
  asm volatile("s_mov_b32 m0, %0\n\ts_nop 0\n\tglobal_load_lds_dwordx4 %1, %2" ::"s"(ldsb), "v"(voff), "s"(base) : "memory");
}
#define WAIT_V(n) asm volatile("s_waitcnt vmcnt(%0)" ::"i"(n) : "memory")
#define WAIT_L0() do { asm volatile("" ::: "memory"); __builtin_amdgcn_s_waitcnt(0xC07F); asm volatile("" ::: "memory"); } while (0)
#define BAR() asm volatile("s_waitcnt lgkmcnt(0)\n\ts_barrier" ::: "memory")
typedef __attribute__((address_space(1))) unsigned long long gu64;
typedef __attribute__((address_space(1))) unsigned gu32;
DEVI void st_gran(char* p, unsigned tag, unsigned v) {
  __hip_atomic_store((gu64*)p, ((unsigned long long)tag << 32) | v, __ATOMIC_RELAXED, __HIP_MEMORY_SCOPE_AGENT);
}
template <int N>
DEVI void sweep(const char* p, int S, unsigned tag, unsigned (&v)[N]) {
  for (int spins = 0; spins < (1 << 16); ++spins) {
    bool ok = true;
#pragma unroll
    for (int k = 0; k < N; ++k) {
      const unsigned long long x = __hip_atomic_load((gu64*)(p + k * S), __ATOMIC_RELAXED, __HIP_MEMORY_SCOPE_AGENT);
      v[k] = (unsigned)x;
      ok &= (unsigned)(x >> 32) == tag;
    }
    if (__all(ok)) break;
    __builtin_amdgcn_s_sleep(8);
  }
}
#define SCHED() __builtin_amdgcn_sched_barrier(0)
DEVI f32x2 halves(float v) {
  const unsigned u = __builtin_bit_cast(unsigned, v);
  auto r = __builtin_amdgcn_permlane32_swap(u, u, false, false);
  const unsigned r0 = r[0], r1 = r[1];
  return f32x2{__builtin_bit_cast(float, r0), __builtin_bit_cast(float, r1)};
}

struct Strm { const char* base; int ld; unsigned voff; };
DEVI Strm mk_strm(const char* base, int ld, int lane) {
  return Strm{base, ld, (unsigned)((lane >> 3) * ld + ((lane & 7) ^ (((lane >> 4) & 3) << 1)) * 16)};
}
DEVI void issue_slab(unsigned slot, const Strm& s, int ks) {
  const char* rb = s.base + (size_t)(ks * 32 * s.ld);
#pragma unroll
  for (int i = 0; i < 4; ++i) dma16(slot + i * 1024, s.voff, rb + (size_t)(i * 8 * s.ld));
}
struct Frag { u32x4 w[4], x[4]; };
template <bool MK>
DEVI void read_frag(Frag& f, const unsigned (&ta)[4], unsigned soff, unsigned xaddr, int tm) {
#pragma unroll
  for (int t = 0; t < 4; ++t)
    if (!MK || (tm >> t & 1)) {
      const u32x2 lo = trd(ta[t] + soff), hi = trd(ta[t] + soff + 2048);
      f.w[t] = u32x4{lo.x, lo.y, hi.x, hi.y};
    }
#pragma unroll
  for (int t = 0; t < 4; ++t) f.x[t] = lrd128(xaddr + t * 1024);
}
template <bool MK, int T0, int T1>
DEVI void mma_part(f32x4 (&acc)[4][4], const Frag& f, int tm) {
#pragma unroll
  for (int t = T0; t < T1; ++t)
    if (!MK || (tm >> t & 1)) {
#pragma unroll
      for (int m = 0; m < 4; ++m) acc[t][m] = mma16(f.w[t], f.x[m], acc[t][m]);
    }
}
DEVI int wrap18(int k) { return k >= 18 ? k - 18 : k; }
template <bool MK>
DEVI void gemm(f32x4 (&acc)[4][4], const unsigned (&ta)[4], unsigned xr0, unsigned xr1, unsigned slot0, const Strm& cur, int k0, int n,
               bool has_next, const Strm& nx, int nk0, int tm = 15) {
  Frag f0, f1;
  if (MK) {
#pragma unroll
    for (int t = 0; t < 4; ++t) f0.w[t] = f1.w[t] = u32x4{0u, 0u, 0u, 0u};
  }
  WAIT_V(4);
  read_frag<MK>(f0, ta, 0, xr0 + k0 * 4096, tm);
  WAIT_L0();
  int k = k0;
  issue_slab(slot0, cur, wrap18(k + 2));
  for (int it = 2; it < n; it += 2) {
    const int k2 = wrap18(k + 2);
    WAIT_V(4);
    read_frag<MK>(f1, ta, 4096, xr1 + k * 4096, tm);
    SCHED();
    mma_part<MK, 0, 2>(acc, f0, tm);
    SCHED();
    WAIT_L0();
    issue_slab(slot0 + 4096, cur, k2 + 1);
    SCHED();
    mma_part<MK, 2, 4>(acc, f0, tm);
    SCHED();
    WAIT_V(4);
    read_frag<MK>(f0, ta, 0, xr0 + k2 * 4096, tm);
    SCHED();
    mma_part<MK, 0, 2>(acc, f1, tm);
    SCHED();
    WAIT_L0();
    if (it + 2 < n) issue_slab(slot0, cur, wrap18(k2 + 2));
    else if (has_next) issue_slab(slot0, nx, nk0);
    SCHED();
    mma_part<MK, 2, 4>(acc, f1, tm);
    SCHED();
    k = k2;
  }
  if (has_next) WAIT_V(4); else WAIT_V(0);
  read_frag<MK>(f1, ta, 4096, xr1 + k * 4096, tm);
  SCHED();
  mma_part<MK, 0, 2>(acc, f0, tm);
  SCHED();
  WAIT_L0();
  if (has_next) issue_slab(slot0 + 4096, nx, nk0 + 1);
  SCHED();
  mma_part<MK, 2, 4>(acc, f0, tm);
  mma_part<MK, 0, 4>(acc, f1, tm);
}

DEVI float mish(float h) {
  float e = __builtin_amdgcn_exp2f(h * 1.4426950408889634f);
  float d = __builtin_fmaf(e, e + 2.0f, 2.0f);
  float r = __builtin_amdgcn_rcpf(d);
  return h * __builtin_fmaf(-2.0f, r, 1.0f);
}

DEVI void set_bias(f32x4 (&acc)[4][4], const u32x2 (&b)[4]) {
#pragma unroll
  for (int t = 0; t < 4; ++t)
#pragma unroll
    for (int m = 0; m < 4; ++m) acc[t][m] = f32x4{bflo(b[t].x), bfhi(b[t].x), bflo(b[t].y), bfhi(b[t].y)};
}
DEVI void set_zero(f32x4 (&acc)[4][4]) {
#pragma unroll
  for (int t = 0; t < 4; ++t)
#pragma unroll
    for (int m = 0; m < 4; ++m) acc[t][m] = f32x4{0.f, 0.f, 0.f, 0.f};
}
template <int H0 = 0, int H1 = 2>
DEVI void mish_pack(const f32x4 (&acc)[4][4], u32x4 (&act)[2][4]) {
#pragma unroll
  for (int h = H0; h < H1; ++h)
#pragma unroll
    for (int m = 0; m < 4; ++m) {
      act[h][m] = u32x4{pk2(mish(acc[2 * h][m][0]), mish(acc[2 * h][m][1])), pk2(mish(acc[2 * h][m][2]), mish(acc[2 * h][m][3])),
                        pk2(mish(acc[2 * h + 1][m][0]), mish(acc[2 * h + 1][m][1])), pk2(mish(acc[2 * h + 1][m][2]), mish(acc[2 * h + 1][m][3]))};
      asm volatile("" : "+v"(act[h][m]));
    }
}
DEVI void write_act(const u32x4 (&act)[2][4], unsigned xr0, unsigned xr1, int blk) {
#pragma unroll
  for (int m = 0; m < 4; ++m) { lwr128(xr0 + blk * 8192 + m * 1024, act[0][m]); lwr128(xr1 + blk * 8192 + m * 1024, act[1][m]); }
}
DEVI void pack_part(const f32x4 (&acc)[4][4], u32x4 (&o)[8]) {
#pragma unroll
  for (int t = 0; t < 4; ++t)
#pragma unroll
    for (int h = 0; h < 2; ++h) {
      o[t * 2 + h] = u32x4{pk2(acc[t][2 * h][0], acc[t][2 * h][1]), pk2(acc[t][2 * h][2], acc[t][2 * h][3]),
                           pk2(acc[t][2 * h + 1][0], acc[t][2 * h + 1][1]), pk2(acc[t][2 * h + 1][2], acc[t][2 * h + 1][3])};
      asm volatile("" : "+v"(o[t * 2 + h]));
    }
}
DEVI void store_part(const u32x4 (&o)[8], char* P, int blk) {
#pragma unroll
  for (int e = 0; e < 8; ++e) __builtin_nontemporal_store(o[e], (u32x4*)(P + ((e & 1) * 36 + blk * 4 + (e >> 1)) * 1024));
}

DEVI void hedgehog(const f32x16& p, unsigned (&out)[10]) {
  float mx = p[0], mn = p[0];
#pragma unroll
  for (int r = 1; r < 9; ++r) { mx = fmaxf(mx, p[r]); mn = fminf(mn, p[r]); }
  { const f32x2 a = halves(mx), b = halves(mn); mx = fmaxf(a.x, a.y); mn = fminf(b.x, b.y); }
  const float L2E = 1.4426950408889634f;
  const float mxl = mx * L2E, mnl = mn * L2E;
  float e[18], sp = 0.f, sn = 0.f;
#pragma unroll
  for (int r = 0; r < 9; ++r) {
    e[r] = __builtin_amdgcn_exp2f(__builtin_fmaf(p[r], L2E, -mxl));
    e[9 + r] = __builtin_amdgcn_exp2f(__builtin_fmaf(p[r], -L2E, mnl));
    sp += e[r]; sn += e[9 + r];
  }
  { const f32x2 a = halves(sp), b = halves(sn); sp = a.x + a.y; sn = b.x + b.y; }
  const float ip = __builtin_amdgcn_rcpf(sp), in = __builtin_amdgcn_rcpf(sn);
#pragma unroll
  for (int r = 0; r < 9; ++r) { e[r] *= ip; e[9 + r] *= in; }
#pragma unroll
  for (int r = 0; r < 9; ++r) out[r] = pk2(e[2 * r], e[2 * r + 1]);
  out[9] = 0u;
}

__global__ __launch_bounds__(512, 2) void k1(const u16* __restrict__ x, const u16* __restrict__ wqkv, const u16* __restrict__ wphi,
                                             const u16* __restrict__ wo, const u16* __restrict__ w1, const u16* __restrict__ b1,
                                             const u16* __restrict__ w2, char* __restrict__ ws) {
  __shared__ __attribute__((aligned(16))) char smem[LDS_BYTES];
  asm volatile("" ::"v"((unsigned)(uintptr_t)(LDS_AS char*)smem));
  const int tid = threadIdx.x, lane = tid & 63, w = __builtin_amdgcn_readfirstlane(tid >> 6);
  const int j = (blockIdx.x & 6) | ((blockIdx.x >> 3) & 1), i = (blockIdx.x >> 4) | ((blockIdx.x & 1) << 4);
  const int c16 = lane & 15, g = lane >> 4;
  const unsigned slot0 = RING_OFF + w * 8192;
  const int r8 = lane >> 3, sub = lane & 7;
  const int mrow = w * 8 + ((r8 >> 1) | ((r8 & 1) << 2));
  u32x4 xv[9];
  {
    const unsigned vo = mrow * 1152 + sub * 16;
    const char* xb = (const char*)x + (size_t)i * 64 * 1152;
#pragma unroll
    for (int c = 0; c < 9; ++c) asm volatile("global_load_dwordx4 %0, %1, %2 offset:%3" : "=v"(xv[c]) : "v"(vo), "s"(xb), "i"(c * 128) : "memory");
  }
  u32x2 bia[4], bi8[4];
  {
    const char* bb = (const char*)b1 + j * 1152;
    const unsigned vo = w * 128 + g * 8, v8 = 1024 + g * 8;
#pragma unroll
    for (int t = 0; t < 4; ++t) {
      asm volatile("global_load_dwordx2 %0, %1, %2 offset:%3" : "=v"(bia[t]) : "v"(vo), "s"(bb), "i"(t * 32) : "memory");
      asm volatile("global_load_dwordx2 %0, %1, %2 offset:%3" : "=v"(bi8[t]) : "v"(v8), "s"(bb), "i"(t * 32) : "memory");
    }
  }
  unsigned tagv;
  asm volatile("global_load_dword %0, %1, %2 sc1" : "=v"(tagv) : "v"(j * 128), "s"(ws + WS_EP) : "memory");
  asm volatile("s_barrier" ::: "memory");
  const int rot = 2 * (i % 9);
  const int part = (w - (w >> 2)) >> 1, line = (w - (w >> 2)) & 1;
  const Strm s1 = mk_strm((const char*)w1 + j * 1152 + w * 128, FF * 2, lane);
  const Strm sr = w == 3 ? mk_strm((const char*)w1 + j * 1152 + 1024, FF * 2, lane)
                         : mk_strm((const char*)wqkv + (part == 0 ? 2304 : (part - 1) * 1152) + (j + line) * 128, 3456, lane);
  if (w < 7) {
    issue_slab(slot0, sr, rot);
    issue_slab(slot0 + 4096, sr, rot + 1);
  } else {
#pragma unroll
    for (int s = 0; s < 3; ++s) { int c = s * 64 + lane; c = c > 161 ? 161 : c; dma16(WPHI_OFF + s * 1024, c * 16, (const char*)wphi + j * 2592); }
#pragma unroll
    for (int s = 0; s < 5; ++s) dma16(DUMMY_OFF, (j * 576 + (i & 15) * 36) * 1152 + (s * 64 + lane) * 128, (const char*)w2);
  }
  asm volatile("s_waitcnt vmcnt(8)" : "+v"(xv[0]), "+v"(xv[1]), "+v"(xv[2]), "+v"(xv[3]), "+v"(xv[4]), "+v"(xv[5]), "+v"(xv[6]), "+v"(xv[7]), "+v"(xv[8]),
               "+v"(bia[0]), "+v"(bia[1]), "+v"(bia[2]), "+v"(bia[3]), "+v"(bi8[0]), "+v"(bi8[1]), "+v"(bi8[2]), "+v"(bi8[3]), "+v"(tagv)::"memory");
  const unsigned tag = __builtin_amdgcn_readfirstlane(tagv) + 1u;
  {
    float s = 0.f, v = 0.f;
    const bf16x2 one2 = __builtin_bit_cast(bf16x2, 0x3f803f80u);
#pragma unroll
    for (int c = 0; c < 9; ++c)
#pragma unroll
      for (int e = 0; e < 4; ++e) {
        const unsigned xu = xv[c][e];
        const bf16x2 xx = __builtin_bit_cast(bf16x2, xu);
        s = __builtin_amdgcn_fdot2_f32_bf16(xx, one2, s, false);
        v = __builtin_amdgcn_fdot2_f32_bf16(xx, xx, v, false);
      }
    s += __shfl_xor(s, 1); v += __shfl_xor(v, 1); s += __shfl_xor(s, 2); v += __shfl_xor(v, 2); s += __shfl_xor(s, 4); v += __shfl_xor(v, 4);
    const float mean = s * (1.0f / 576.0f);
    const float var = fmaxf(__builtin_fmaf(-mean, mean, v * (1.0f / 576.0f)), 0.f);
    const float rs = __builtin_amdgcn_rsqf(var + 1e-5f), nb = -mean * rs;
    const int f = ((sub >> 2) << 1) + (sub & 1);
    const unsigned wa = XA_OFF + (sub >> 2) * 4096 + (mrow >> 4) * 1024 + ((sub & 1) * 32 + ((mrow & 15) ^ f)) * 16 + ((sub >> 1) & 1) * 8;
#pragma unroll
    for (int c = 0; c < 9; ++c) {
      unsigned o[4];
#pragma unroll
      for (int e = 0; e < 4; ++e) o[e] = pk2(__builtin_fmaf(bflo(xv[c][e]), rs, nb), __builtin_fmaf(bfhi(xv[c][e]), rs, nb));
      lwr64(wa + c * 8192, u32x2{o[0], o[1]});
      lwr64(wa + c * 8192 + 256, u32x2{o[2], o[3]});
    }
  }
  BAR();
  const unsigned xr0 = XA_OFF + (g * 16 + (c16 ^ (g >> 1))) * 16, xr1 = XA_OFF + 4096 + (g * 16 + (c16 ^ (2 + (g >> 1)))) * 16;
  unsigned ta[4];
  {
    const int q = (lane >> 2) & 3, p = lane & 3, r = 4 * g + q, s = (r >> 1) & 3;
#pragma unroll
    for (int t = 0; t < 4; ++t) ta[t] = slot0 + 128 * r + 32 * (t ^ s) + 8 * p;
  }
  f32x4 acc[4][4];
  if (w == 3) set_bias(acc, bi8); else set_zero(acc);
  if (w < 7) {
    const int tm = w == 3 ? 15 : (line == 0 ? (15 << (j >> 1)) & 15 : 15 >> (3 - (j >> 1)));
    gemm<true>(acc, ta, xr0, xr1, slot0, sr, rot, 18, w < 4, s1, rot, tm);
  } else {
    dma16(DUMMY_OFF, (j * 576 + (i & 15) * 36) * 1152 + (320 + (lane & 3)) * 128, (const char*)w2);
#pragma unroll
    for (int s = 0; s < 3; ++s) {
      const int fr = blockIdx.x + s * 256;
      if (fr < 648) {
        const int ks = fr / 36, nt = fr - ks * 36;
        const int ch = ks * 4 + g, c9 = ch % 9;
        const u16* src = wo + ((ch / 9) * 72 + (c9 < 8 ? 9 * c9 : 8)) * 576 + nt * 16 + c16;
        const int st = c9 < 8 ? 576 : 9 * 576;
        u32x4 o;
#pragma unroll
        for (int e = 0; e < 4; ++e) o[e] = (unsigned)src[(2 * e) * st] | ((unsigned)src[(2 * e + 1) * st] << 16);
        *(u32x4*)(ws + WS_WOT + ((size_t)fr * 64 + lane) * 16) = o;
      }
    }
    WAIT_V(0);
    const int r32 = lane & 31, reg = (r32 & 3) + 4 * (r32 >> 3), f = 9 * ((r32 >> 2) & 1) + reg;
#pragma nounroll
    for (int u = 0; u < 8; ++u) {
      const int hh = u >> 1;
      u32x4 wp;
#pragma unroll
      for (int e = 0; e < 4; ++e) {
        unsigned v2[2];
#pragma unroll
        for (int b = 0; b < 2; ++b) {
          const int d = 16 * (u & 1) + 8 * (lane >> 5) + 2 * e + b - 2 * hh;
          const bool ok = reg < 9 && d >= 0 && d < 18;
          const unsigned val = *(LDS_AS u16*)(uintptr_t)(WPHI_OFF + (ok ? (hh * 324 + d * 18 + f) * 2 : 0));
          v2[b] = ok ? val : 0u;
        }
        wp[e] = v2[0] | (v2[1] << 16);
      }
      lwr128(PH_OFF + hh * 5120 + (u & 1) * 1024 + lane * 16, wp);
    }
  }
  u32x4 act8[2][4];
  BAR();
  if (w == 7) {
    lwr128(S2_OFF + lane * S2_ROW + 432, u32x4{0u, 0u, 0u, 0u});
    *(LDS_AS unsigned*)(uintptr_t)(WPHI_OFF + 3068) = 0u;
  } else if (w != 3) {
#pragma unroll
    for (int t = 0; t < 4; ++t) {
      const int lc = 64 * line + 16 * t + 4 * g - 8 * j;
      if (lc >= 0 && lc < 72) {
#pragma unroll
        for (int m = 0; m < 4; ++m)
          lwr64(S2_OFF + (m * 16 + c16) * S2_ROW + part * 144 + lc * 2, u32x2{pk2(acc[t][m][0], acc[t][m][1]), pk2(acc[t][m][2], acc[t][m][3])});
      }
    }
  } else {
    mish_pack<0, 1>(acc, act8);
  }
  BAR();
  const Strm s2 = mk_strm((const char*)w2 + (size_t)j * 576 * 1152 + w * 128, DM * 2, lane);
  char* P = ws + WS_P + ((size_t)(j * 64 + 2 * i) * 36 * 64 + lane) * 16;
  u32x4 act[2][4];
  if (w < 4) {
    if (w == 3) mish_pack<1, 2>(acc, act8);
    set_bias(acc, bia);
    gemm<false>(acc, ta, xr0, xr1, slot0, s1, rot, 18, true, s2, 16);
    mish_pack(acc, act);
    BAR();
    write_act(act, xr0, xr1, w);
    if (w == 3) write_act(act8, xr0, xr1, 8);
    BAR();
    set_zero(acc);
    gemm<false>(acc, ta, xr0, xr1, slot0, s2, 16, 10, true, s2, 8);
    BAR();
    if (w != 3) {
      const int col = i * 48 + w * 16 + c16;
      unsigned pv[8];
      sweep<8>(ws + WS_KVP + ((size_t)(j * 32 + g * 8) * 1536 + (col < 1520 ? col : 0)) * 8, 1536 * 8, tag, pv);
      float a = 0.f, b = 0.f;
#pragma unroll
      for (int e = 0; e < 8; ++e) { a += bflo(pv[e]); b += bfhi(pv[e]); }
      a += __shfl_xor(a, 16); b += __shfl_xor(b, 16); a += __shfl_xor(a, 32); b += __shfl_xor(b, 32);
      if (g == 0) st_gran(ws + WS_KV + (size_t)(j * 1536 + col) * 8, tag, pk2(a, b));
      if (i == 0 && w == 1 && lane == 0) __hip_atomic_store((gu32*)(ws + WS_EP + j * 128), tag, __ATOMIC_RELAXED, __HIP_MEMORY_SCOPE_AGENT);
    }
    const Strm s8 = mk_strm((const char*)w2 + (size_t)j * 576 * 1152 + 1024, DM * 2, lane);
    gemm<false>(acc, ta, xr0, xr1, slot0, s2, 8, 8, true, s8, 4 * w);
    u32x4 po[8], p8[8];
    pack_part(acc, po);
    set_zero(acc);
    gemm<false>(acc, ta, xr0, xr1, slot0, s8, 4 * w, w == 3 ? 6 : 4, false, s8, 0);
    store_part(po, P, w);
    pack_part(acc, p8);
#pragma unroll
    for (int e = 0; e < 8; ++e) lwr128(slot0 + e * 1024 + lane * 16, p8[e]);
    {
      const int r32 = lane & 31, hf = lane >> 5, reg = (r32 & 3) + 4 * (r32 >> 3);
      const int d = reg < 9 ? 9 * ((r32 >> 2) & 1) + reg : 18;
      unsigned kq[10], fq[2][10];
#pragma unroll
      for (int mh = 0; mh < 2; ++mh) {
        f32x16 p;
#pragma unroll
        for (int e = 0; e < 5; ++e) {
          const u32x2 u = lrd64(PH_OFF + w * 5120 + (mh * 32 + r32) * 80 + hf * 40 + e * 8);
          const unsigned a = u.x, b = u.y;
          p[2 * e] = __uint_as_float(a); p[2 * e + 1] = __uint_as_float(b);
        }
        hedgehog(p, fq[mh]);
      }
      sweep<10>(ws + WS_KV + (size_t)(j * 1536 + w * 380 + d * 20 + hf * 10) * 8, 8, tag, kq);
      const int ch = 9 * j + 2 * w + hf, c8 = 9 * j + 8;
#pragma unroll
      for (int mh = 0; mh < 2; ++mh) {
        f32x16 o = {0.f, 0.f, 0.f, 0.f, 0.f, 0.f, 0.f, 0.f, 0.f, 0.f, 0.f, 0.f, 0.f, 0.f, 0.f, 0.f};
        o = mma32(u32x4{kq[0], kq[1], kq[2], kq[3]}, u32x4{fq[mh][0], fq[mh][1], fq[mh][2], fq[mh][3]}, o);
        o = mma32(u32x4{kq[4], kq[5], kq[6], kq[7]}, u32x4{fq[mh][4], fq[mh][5], fq[mh][6], fq[mh][7]}, o);
        o = mma32(u32x4{kq[8], kq[9], 0u, 0u}, u32x4{fq[mh][8], fq[mh][9], 0u, 0u}, o);
        const float inv = 1.0f / (o[9] + 1e-6f);
        char* ob = ws + WS_O + ((size_t)(2 * i + mh) * 36 + (r32 >> 4)) * 1024 + (r32 & 15) * 16;
        *(u32x4*)(ob + (ch >> 2) * 2048 + (ch & 3) * 256) =
            u32x4{pk2(o[0] * inv, o[1] * inv), pk2(o[2] * inv, o[3] * inv), pk2(o[4] * inv, o[5] * inv), pk2(o[6] * inv, o[7] * inv)};
        *(u16*)(ob + (c8 >> 2) * 2048 + (c8 & 3) * 256 + (2 * w + hf) * 2) = (u16)pk2(o[8] * inv, 0.f);
      }
    }
  } else {
    const int hh = w & 3, r32 = lane & 31, hf = lane >> 5;
    __builtin_amdgcn_s_setprio(2);
    const u32x4 wp[2] = {lrd128(PH_OFF + hh * 5120 + lane * 16), lrd128(PH_OFF + hh * 5120 + 1024 + lane * 16)};
    f32x16 pk[2], pq[2];
    u32x4 vf[2][2];
    const f32x16 z16 = {0.f, 0.f, 0.f, 0.f, 0.f, 0.f, 0.f, 0.f, 0.f, 0.f, 0.f, 0.f, 0.f, 0.f, 0.f, 0.f};
#pragma unroll
    for (int mh = 0; mh < 2; ++mh) {
      const unsigned rowa = S2_OFF + (mh * 32 + r32) * S2_ROW + hh * 32 + hf * 16;
      pk[mh] = mma32(wp[1], lrd128(rowa + 288 + 32), mma32(wp[0], lrd128(rowa + 288), z16));
      pq[mh] = mma32(wp[1], lrd128(rowa + 144 + 32), mma32(wp[0], lrd128(rowa + 144), z16));
      const unsigned va = S2_OFF + (mh * 32 + 8 * g + ((lane >> 2) & 3)) * S2_ROW + 8 * (lane & 3) + hh * 32;
#pragma unroll
      for (int dt = 0; dt < 2; ++dt) {
        const u32x2 v0 = trd(va + dt * 32), v1 = trd(va + 4 * S2_ROW + dt * 32);
        vf[mh][dt] = u32x4{v0.x, v0.y, v1.x, v1.y};
      }
    }
    WAIT_L0();
    LDS_AS unsigned* cnt = (LDS_AS unsigned*)(uintptr_t)(WPHI_OFF + 3068);
    if (lane == 0) __hip_atomic_fetch_add(cnt, 1u, __ATOMIC_RELAXED, __HIP_MEMORY_SCOPE_WORKGROUP);
#pragma unroll
    for (int mh = 0; mh < 2; ++mh) {
      unsigned ph[10];
      hedgehog(pk[mh], ph);
#pragma unroll
      for (int e = 0; e < 5; ++e) lwr64(PH_OFF + hh * 5120 + (mh * 32 + r32) * 80 + hf * 40 + e * 8, u32x2{ph[2 * e], ph[2 * e + 1]});
    }
    while (__builtin_amdgcn_readfirstlane(__hip_atomic_load(cnt, __ATOMIC_RELAXED, __HIP_MEMORY_SCOPE_WORKGROUP)) < 4u) __builtin_amdgcn_s_sleep(1);
    issue_slab(slot0, s1, rot);
    issue_slab(slot0 + 4096, s1, rot + 1);
    {
      const int q = (lane >> 2) & 3, p = lane & 3;
      const unsigned pa = PH_OFF + hh * 5120 + (8 * g + q) * 80 + 8 * p;
      f32x4 kv[2][3];
#pragma unroll
      for (int e = 0; e < 6; ++e) kv[e / 3][e % 3] = f32x4{0.f, 0.f, 0.f, 0.f};
#pragma unroll
      for (int s = 0; s < 2; ++s) {
        if (c16 == 15) vf[s][1] = u32x4{0x3f803f80u, 0x3f803f80u, 0x3f803f80u, 0x3f803f80u};
#pragma unroll
        for (int ft = 0; ft < 3; ++ft) {
          const u32x2 p0 = trd(pa + s * 32 * 80 + ft * 32), p1 = trd(pa + (s * 32 + 4) * 80 + ft * 32);
          const u32x4 pf = {p0.x, p0.y, p1.x, p1.y};
          kv[0][ft] = mma16(pf, vf[s][0], kv[0][ft]);
          kv[1][ft] = mma16(pf, vf[s][1], kv[1][ft]);
        }
      }
#pragma unroll
      for (int dt = 0; dt < 2; ++dt) {
        const int col = dt * 16 + c16, d = col == 31 ? 18 : col - 2 * hh;
        if (d >= 0 && (d < 18 || col == 31)) {
          char* dst = ws + WS_KVP + ((size_t)(j * 32 + i) * 1536 + hh * 380 + d * 20 + 2 * g) * 8;
#pragma unroll
          for (int ft = 0; ft < 3; ++ft)
            if (ft < 2 || g < 2) {
              const u32x4 gr = {pk2(kv[dt][ft][0], kv[dt][ft][1]), tag, pk2(kv[dt][ft][2], kv[dt][ft][3]), tag};
              asm volatile("global_store_dwordx4 %0, %1, off offset:%2 sc1\n\ts_nop 1" ::"v"(dst), "v"(gr), "i"(ft * 64) : "memory");
            }
        }
      }
#pragma unroll
      for (int mh = 0; mh < 2; ++mh)
#pragma unroll
        for (int e = 0; e < 5; ++e) {
          const float a = pq[mh][2 * e], b = pq[mh][2 * e + 1];
          lwr64(PH_OFF + hh * 5120 + (mh * 32 + r32) * 80 + hf * 40 + e * 8, u32x2{__float_as_uint(a), __float_as_uint(b)});
        }
    }
    __builtin_amdgcn_s_setprio(0);
    set_bias(acc, bia);
    gemm<false>(acc, ta, xr0, xr1, slot0, s1, rot, 18, true, s2, 0);
    BAR();
    BAR();
    mish_pack(acc, act);
    write_act(act, xr0, xr1, w);
    BAR();
    set_zero(acc);
    gemm<false>(acc, ta, xr0, xr1, slot0, s2, 0, 18, false, s2, 0);
    u32x4 po[8];
    pack_part(acc, po);
    store_part(po, P, w);
  }
  BAR();
  {
    float v[8] = {0.f, 0.f, 0.f, 0.f, 0.f, 0.f, 0.f, 0.f};
#pragma unroll
    for (int a = 0; a < 4; ++a) {
      const u32x4 u = lrd128(RING_OFF + a * 8192 + w * 1024 + lane * 16);
#pragma unroll
      for (int e = 0; e < 4; ++e) { v[2 * e] += bflo(u[e]); v[2 * e + 1] += bfhi(u[e]); }
    }
    *(u32x4*)(P + ((w & 1) * 36 + 32 + (w >> 1)) * 1024) = u32x4{pk2(v[0], v[1]), pk2(v[2], v[3]), pk2(v[4], v[5]), pk2(v[6], v[7])};
  }
}

__global__ __launch_bounds__(576) void k2(const u16* __restrict__ x, const char* __restrict__ ws, u16* __restrict__ out) {
  __shared__ __attribute__((aligned(16))) char ob[36864];
  const int tid = threadIdx.x, lane = tid & 63, w = __builtin_amdgcn_readfirstlane(tid >> 6);
  const int b = blockIdx.x, i32 = (b & 7) + 8 * (b >> 5), nt = ((b >> 3) & 3) * 9 + w;
  const int c16 = lane & 15, g = lane >> 4;
  const unsigned obase = (unsigned)(uintptr_t)(LDS_AS char*)ob;
#pragma unroll
  for (int s = 0; s < 4; ++s) dma16(obase + (w * 4 + s) * 1024, lane * 16, ws + WS_O + ((size_t)i32 * 36 + w * 4 + s) * 1024);
  u32x4 wf[18], pp[8];
  u32x2 xr[2];
#pragma unroll
  for (int s = 0; s < 8; ++s) pp[s] = *(const u32x4*)(ws + WS_P + ((size_t)((s * 64 + i32) * 36 + nt) * 64 + lane) * 16);
  asm volatile("" ::: "memory");
#pragma unroll
  for (int ks = 0; ks < 18; ++ks) wf[ks] = *(const u32x4*)(ws + WS_WOT + ((size_t)(ks * 36 + nt) * 64 + lane) * 16);
#pragma unroll
  for (int m = 0; m < 2; ++m) xr[m] = *(const u32x2*)(x + (size_t)(i32 * 32 + m * 16 + c16) * 576 + nt * 16 + g * 4);
  WAIT_V(28);
  BAR();
  u32x4 of[12];
#pragma unroll
  for (int e = 0; e < 12; ++e) of[e] = lrd128(obase + e * 1024 + lane * 16);
  SCHED();
  float gt[2][4];
#pragma unroll
  for (int m = 0; m < 2; ++m) {
    float bs[4] = {0.f, 0.f, 0.f, 0.f};
#pragma unroll
    for (int s = 0; s < 8; ++s) {
      bs[0] += bflo(pp[s][2 * m]); bs[1] += bfhi(pp[s][2 * m]); bs[2] += bflo(pp[s][2 * m + 1]); bs[3] += bfhi(pp[s][2 * m + 1]);
    }
#pragma unroll
    for (int r = 0; r < 4; ++r) gt[m][r] = bs[r] * __builtin_amdgcn_rcpf(1.0f + __builtin_amdgcn_exp2f(-1.4426950408889634f * bs[r]));
    asm volatile("" : "+v"(gt[m][0]), "+v"(gt[m][1]), "+v"(gt[m][2]), "+v"(gt[m][3]));
  }
  SCHED();
  f32x4 acc[2] = {{0.f, 0.f, 0.f, 0.f}, {0.f, 0.f, 0.f, 0.f}};
#pragma unroll
  for (int e = 0; e < 36; ++e) {
    acc[e & 1] = mma16(wf[e >> 1], of[e % 12], acc[e & 1]);
    if (e < 24) of[e % 12] = lrd128(obase + (e + 12) * 1024 + lane * 16);
    SCHED();
  }
#pragma unroll
  for (int m = 0; m < 2; ++m) {
    const float xs[4] = {bflo(xr[m].x), bfhi(xr[m].x), bflo(xr[m].y), bfhi(xr[m].y)};
    float o[4];
#pragma unroll
    for (int r = 0; r < 4; ++r) o[r] = __builtin_fmaf(acc[m][r], gt[m][r], xs[r]);
    *(u32x2*)(out + (size_t)(i32 * 32 + m * 16 + c16) * 576 + nt * 16 + g * 4) = u32x2{pk2(o[0], o[1]), pk2(o[2], o[3])};
  }
}

extern "C" void kernel_launch(void* const* d_in, const int* in_sizes, int n_in, void* d_out, int out_size, void* d_ws, size_t ws_size,
                              hipStream_t stream) {
  const u16* x = (const u16*)d_in[0];
  const u16* wqkv = (const u16*)d_in[1];
  const u16* wphi = (const u16*)d_in[2];
  const u16* wo = (const u16*)d_in[3];
  const u16* w1 = (const u16*)d_in[5];
  const u16* b1 = (const u16*)d_in[6];
  const u16* w2 = (const u16*)d_in[7];
  char* ws = (char*)d_ws;
  k1<<<256, 512, 0, stream>>>(x, wqkv, wphi, wo, w1, b1, w2, ws);
  k2<<<256, 576, 0, stream>>>(x, ws, (u16*)d_out);
}
```
